```python
import torch

def _softmax(x, axis=-1):
    return torch.softmax(x, dim=axis)
def _gelu(x):
    return torch.nn.functional.gelu(x, approximate="tanh")
def _silu(x):
    return torch.nn.functional.silu(x)
def _sigmoid(x):
    return torch.sigmoid(x)
def _layernorm(x, axis=-1, eps=1e-5):
    m = x.mean(dim=axis, keepdim=True)
    v = x.var(dim=axis, keepdim=True, unbiased=False)
    return (x - m) / torch.sqrt(v + eps)
def _scatter_add(tgt, idx, src, axis=0):
    return tgt.clone().index_add_(axis, idx, src)
def _segment_sum(src, seg, n):
    out = torch.zeros((n,) + src.shape[1:], dtype=src.dtype)
    return out.index_add_(0, seg, src)
def _pad(x, pw):
    flat = tuple(v for pair in reversed(pw) for v in pair)
    return torch.nn.functional.pad(x, flat)

kernel_name = 'parallel_sum__sliding_block_cached__mixer_channel_mlp__decode'

def setup_inputs():

    _w = (lambda a: (lambda t: t.to(torch.bfloat16) if t.is_floating_point() else t)(torch.as_tensor(a)))
    def _A_setup():
        B, Sq, D = 1, 1, 1024
        Hq, Hkv, Sk, Dh = 32, 4, 65536, 128
        assert Hq % Hkv == 0, (Hq, Hkv)
        return {
            "x": _w((lambda *s: torch.randn(*s, dtype=torch.bfloat16) * 0.05)(B, Sq, D)),
            "k_cache": _w((lambda *s: torch.randn(*s, dtype=torch.bfloat16) * 3.2)(B, Hkv, Sk, Dh)),
            "v_cache": _w((lambda *s: torch.randn(*s, dtype=torch.bfloat16) * 0.4)(B, Hkv, Sk, Dh)),
            "wq": _w((lambda *s: torch.randn(*s, dtype=torch.bfloat16) * 3.2)(D, Hq * Dh) * (D ** -0.5)),
            "wo": _w((lambda *s: torch.randn(*s, dtype=torch.bfloat16) * 0.4)(Hq * Dh, D) * ((Hq * Dh) ** -0.5)),
        }
    _A_in = _A_setup()

    def _B_setup():
        B, S, D, F = 1, 1, 1024, 3584
        return {
            "x": _w((lambda *s: torch.randn(*s, dtype=torch.bfloat16) * 0.05)(B, S, D)),
            "w1": _w((lambda *s: torch.randn(*s, dtype=torch.bfloat16) * 1.6)(D, F) * (D ** -0.5)),
            "b1": _w((lambda *s: torch.randn(*s, dtype=torch.bfloat16) * 1.6)(F)),
            "w2": _w((lambda *s: torch.randn(*s, dtype=torch.bfloat16) * 0.1)(F, D) * (F ** -0.5)),
            "b2": _w((lambda *s: torch.randn(*s, dtype=torch.bfloat16) * 0.1)(D)),
        }
    _B_in = _B_setup()

    return {
        "A_x": _A_in["x"],
        "A_k_cache": _A_in["k_cache"],
        "A_v_cache": _A_in["v_cache"],
        "A_wq": _A_in["wq"],
        "A_wo": _A_in["wo"],
        "B_x": _B_in["x"],
        "B_w1": _B_in["w1"],
        "B_b1": _B_in["b1"],
        "B_w2": _B_in["w2"],
        "B_b2": _B_in["b2"],
    }

def reference(A_x, A_k_cache, A_v_cache, A_wq, A_wo, B_x, B_w1, B_b1, B_w2, B_b2):
    def _A_ref(x, k_cache, v_cache, wq, wo):
        B, Sq, D = x.shape
        Hq, Dh = 32, 128
        Hkv, Sk = k_cache.shape[1], k_cache.shape[2]
        W = min(4096, max(256, Sk // 4))
        rep = Hq // Hkv
        q = (lambda x, *d: x.permute(*d))((x @ wq).reshape(B, Sq, Hq, Dh), 0, 2, 1, 3)
        k = (lambda x, n, axis: torch.repeat_interleave(x, n, dim=axis))(k_cache[:, :, Sk - W:, :], rep, axis=1)
        v = (lambda x, n, axis: torch.repeat_interleave(x, n, dim=axis))(v_cache[:, :, Sk - W:, :], rep, axis=1)
        scores = torch.einsum("bhqd,bhkd->bhqk", q, k) * (Dh ** -0.5)
        p = _softmax(scores, axis=-1)
        o = torch.einsum("bhqk,bhkd->bhqd", p, v)
        return (lambda x, *d: x.permute(*d))(o, 0, 2, 1, 3).reshape(B, Sq, Hq * Dh) @ wo

    def _B_ref(x, w1, b1, w2, b2):
        y = _layernorm(x, axis=-1)
        h = _gelu(torch.einsum("bsd,df->bsf", y, w1) + b1)
        return torch.einsum("bsf,fd->bsd", h, w2) + b2

    x = A_x
    xn = _layernorm(x, axis=-1)
    return x + _A_ref(xn, A_k_cache, A_v_cache, A_wq, A_wo) + _B_ref(xn, B_w1, B_b1, B_w2, B_b2)
```

```cpp
#include <hip/hip_runtime.h>
#include <hip/hip_bf16.h>
#include <stdint.h>
#define MIMIC 0
typedef unsigned short u16;
__device__ __forceinline__ float bf2f(u16 b) { return __uint_as_float(((unsigned)b) << 16); }
__device__ __forceinline__ u16 f2bf(float f) { __hip_bfloat16 h = __float2bfloat16(f); return *reinterpret_cast<u16*>(&h); }
__device__ __forceinline__ float rb(float f) {
  return f;
}
constexpr int D = 1024, HQ = 32, HKV = 4, SK = 65536, DH = 128, W = 4096, F = 3584;

__device__ float block_sum(float v, float* sh) {
  for (int o = 32; o >= 1; o >>= 1) v += __shfl_xor(v, o);
  int w = threadIdx.x >> 6;
  __syncthreads();
  if ((threadIdx.x & 63) == 0) sh[w] = v;
  __syncthreads();
  float s = 0;
  for (int i = 0; i < (int)(blockDim.x >> 6); ++i) s += sh[i];
  return s;
}
__device__ void do_ln(const u16* x, float* sx, float* sy, float* sh) {
  int t = threadIdx.x;
  float v[4]; float s = 0;
  for (int i = 0; i < 4; ++i) { v[i] = bf2f(x[t * 4 + i]); s += v[i]; }
  float mean = block_sum(s, sh) / D;
  float q = 0; for (int i = 0; i < 4; ++i) { float d = v[i] - mean; q += d * d; }
  float var = block_sum(q, sh) / D;
  float mb = rb(mean), vb = rb(var);
  float den = rb(sqrtf(rb(vb + 1e-5f)));
  float xn[4]; s = 0;
  for (int i = 0; i < 4; ++i) { xn[i] = rb(rb(v[i] - mb) / den); sx[t * 4 + i] = xn[i]; s += xn[i]; }
  mean = block_sum(s, sh) / D;
  q = 0; for (int i = 0; i < 4; ++i) { float d = xn[i] - mean; q += d * d; }
  var = block_sum(q, sh) / D;
  mb = rb(mean); vb = rb(var);
  den = rb(sqrtf(rb(vb + 1e-5f)));
  for (int i = 0; i < 4; ++i) { sy[t * 4 + i] = rb(rb(xn[i] - mb) / den); }
  __syncthreads();
}
__global__ void k1(const u16* x, const u16* wq, const u16* w1, const u16* b1, float* q, float* h) {
  __shared__ float sx[D], sy[D], sh[8];
  do_ln(x, sx, sy, sh);
  int col = blockIdx.x * 256 + threadIdx.x;
  if (col < HQ * DH) {
    float a = 0;
    for (int k = 0; k < D; ++k) a += sx[k] * bf2f(wq[(size_t)k * (HQ * DH) + col]);
    q[col] = rb(a);
  } else {
    int f = col - HQ * DH;
    float a = 0;
    for (int k = 0; k < D; ++k) a += sy[k] * bf2f(w1[(size_t)k * F + f]);
    a = rb(rb(a) + bf2f(b1[f]));
    float u = 0.7978845608028654f * (a + 0.044715f * a * a * a);
    float g = 0.5f * a * (1.0f + tanhf(u));
    h[f] = rb(g);
  }
}
__global__ void k2(const float* q, const u16* kc, const u16* vc, float* o) {
  __shared__ float sc[W]; __shared__ float sq[DH]; __shared__ float sh[8];
  int hd = blockIdx.x, g = hd / (HQ / HKV), t = threadIdx.x;
  if (t < DH) sq[t] = q[hd * DH + t];
  __syncthreads();
  const u16* K = kc + ((size_t)g * SK + (SK - W)) * DH;
  const u16* V = vc + ((size_t)g * SK + (SK - W)) * DH;
  float mx = -INFINITY;
  for (int p = t; p < W; p += 256) {
    float a = 0;
    for (int d = 0; d < DH; ++d) a += sq[d] * bf2f(K[(size_t)p * DH + d]);
    a = rb(rb(a) * 0.08838834764831845f);
    sc[p] = a; mx = fmaxf(mx, a);
  }
  for (int o2 = 32; o2 >= 1; o2 >>= 1) mx = fmaxf(mx, __shfl_xor(mx, o2));
  if ((t & 63) == 0) sh[t >> 6] = mx;
  __syncthreads();
  mx = fmaxf(fmaxf(sh[0], sh[1]), fmaxf(sh[2], sh[3]));
  float s = 0;
  for (int p = t; p < W; p += 256) { float e = expf(sc[p] - mx); sc[p] = e; s += e; }
  s = block_sum(s, sh);
  for (int p = t; p < W; p += 256) sc[p] = rb(sc[p] / s);
  __syncthreads();
  if (t < DH) {
    float a = 0;
    for (int p = 0; p < W; ++p) a += sc[p] * bf2f(V[(size_t)p * DH + t]);
    o[hd * DH + t] = rb(a);
  }
}
__global__ void k3(const u16* x, const float* o, const float* h, const u16* wo, const u16* w2, const u16* b2, u16* out) {
  int c = blockIdx.x * 64 + threadIdx.x;
  float a = 0;
  for (int k = 0; k < HQ * DH; ++k) a += o[k] * bf2f(wo[(size_t)k * D + c]);
  float b = 0;
  for (int k = 0; k < F; ++k) b += h[k] * bf2f(w2[(size_t)k * D + c]);
  a = rb(a); b = rb(rb(b) + bf2f(b2[c]));
  float r = rb(bf2f(x[c]) + a) + b;
  out[c] = f2bf(r);
}
extern "C" void kernel_launch(void* const* d_in, const int* in_sizes, int n_in, void* d_out, int out_size, void* d_ws, size_t ws_size, hipStream_t stream) {
  const u16* x = (const u16*)d_in[0]; const u16* kc = (const u16*)d_in[1]; const u16* vc = (const u16*)d_in[2];
  const u16* wq = (const u16*)d_in[3]; const u16* wo = (const u16*)d_in[4];
  const u16* w1 = (const u16*)d_in[6]; const u16* b1 = (const u16*)d_in[7]; const u16* w2 = (const u16*)d_in[8]; const u16* b2 = (const u16*)d_in[9];
  float* q = (float*)d_ws; float* h = q + 4096; float* o = h + 4096;
  k1<<<(4096 + F) / 256, 256, 0, stream>>>(x, wq, w1, b1, q, h);
  k2<<<HQ, 256, 0, stream>>>(q, kc, vc, o);
  k3<<<D / 64, 64, 0, stream>>>(x, o, h, wo, w2, b2, (u16*)d_out);
}
```

```cpp
#include <hip/hip_runtime.h>
#include <stdint.h>

#define GAS __attribute__((address_space(1)))
#define LAS __attribute__((address_space(3)))
typedef unsigned u32;
typedef unsigned long long u64;
typedef unsigned char u8;
typedef u32 v4u __attribute__((ext_vector_type(4)));
typedef u32 v2u __attribute__((ext_vector_type(2)));
typedef float f4 __attribute__((ext_vector_type(4)));
typedef float f2 __attribute__((ext_vector_type(2)));
typedef short bf16x8 __attribute__((ext_vector_type(8)));
typedef __bf16 bf16x2 __attribute__((ext_vector_type(2)));

#define STAMPS 0
#define PW 6

namespace {
constexpr int SK = 65536, WIN = 4096;
constexpr int NWAVES = 9, NTHR = NWAVES * 64;
constexpr u32 WS_EPOCH = 0;
constexpr u32 WS_QP = 4096;
constexpr u32 WS_HP = WS_QP + 4 * 4096 * 8;
constexpr u32 WS_ML = WS_HP + 4 * 3584 * 8;
constexpr u32 WS_OP = WS_ML + 32 * 64 * 16;
constexpr u32 WS_FP = WS_OP + 32 * 2 * 64 * 64 * 8;
constexpr u32 WS_END = WS_FP + 2 * 64 * 1024 * 8;
constexpr u32 WS_STAMP = 8u << 20;
constexpr int L_K = 0, L_V = 16384, L_XS = 32768, L_YS = L_XS + 512, L_QPART = L_YS + 512, L_HPART = L_QPART + 1024,
              L_QIMG = L_HPART + 1024, L_SC = L_QIMG + 2304, L_MLX = L_SC + 2304, L_OIMG = L_MLX + 256, L_HIMG = L_OIMG + 128,
              L_CTRL = L_HIMG + 128, L_USED = L_CTRL + 256, L_BYTES = 90112;
static_assert(L_USED <= L_BYTES, "lds");
enum { C_LN = 0, C_LY, C_QP, C_QD, C_KV, C_HP, C_HD, C_Q, C_S, C_O, C_H, C_FB, C_T32, C_T8, C_EHI };
constexpr u32 SPIN_LDS = 1u << 22, SPIN_GLB = 1u << 18;

struct Params { const u8 *x, *kc, *vc, *wq, *wo, *w1, *b1, *w2, *b2; u8 *out, *ws; };

__device__ __forceinline__ float bf2f(u32 b) { return __uint_as_float(b << 16); }
__device__ __forceinline__ u32 pkbf(float lo, float hi) {
  bf16x2 v = {(__bf16)lo, (__bf16)hi};
  return __builtin_bit_cast(u32, v);
}
__device__ __forceinline__ float rbf(float f) { return (float)(__bf16)f; }
__device__ __forceinline__ float dot2(u32 a, u32 b, float c) {
  return __builtin_amdgcn_fdot2_f32_bf16(__builtin_bit_cast(bf16x2, a), __builtin_bit_cast(bf16x2, b), c, false);
}
__device__ __forceinline__ u32 plo(u32 a, u32 b) { return __builtin_amdgcn_perm(b, a, 0x05040100u); }
__device__ __forceinline__ u32 phi(u32 a, u32 b) { return __builtin_amdgcn_perm(b, a, 0x07060302u); }
__device__ __forceinline__ f4 mfma(v4u a, v4u b, f4 c) {
  return __builtin_amdgcn_mfma_f32_16x16x32_bf16(__builtin_bit_cast(bf16x8, a), __builtin_bit_cast(bf16x8, b), c, 0, 0, 0);
}
template <int CTRL> __device__ __forceinline__ float dpp(float x) {
  return __builtin_bit_cast(float, __builtin_amdgcn_mov_dpp(__builtin_bit_cast(int, x), CTRL, 0xf, 0xf, true));
}
constexpr int XOR1 = 0xB1, XOR2 = 0x4E, HMIR = 0x141, ROR8 = 0x128, ROR4 = 0x124;
__device__ __forceinline__ float tr32(float a, float b) {
  auto s = __builtin_amdgcn_permlane32_swap(__float_as_uint(a), __float_as_uint(b), false, false);
  return __uint_as_float(s[0]) + __uint_as_float(s[1]);
}
__device__ __forceinline__ float tr16(float a, float b) {
  auto s = __builtin_amdgcn_permlane16_swap(__float_as_uint(a), __float_as_uint(b), false, false);
  return __uint_as_float(s[0]) + __uint_as_float(s[1]);
}
__device__ __forceinline__ float row_sum(float v) {
  v += dpp<XOR1>(v); v += dpp<XOR2>(v); v += dpp<HMIR>(v); v += dpp<ROR8>(v);
  return v;
}
__device__ __forceinline__ float row_max(float v) {
  v = fmaxf(v, dpp<XOR1>(v)); v = fmaxf(v, dpp<XOR2>(v)); v = fmaxf(v, dpp<HMIR>(v)); v = fmaxf(v, dpp<ROR8>(v));
  return v;
}
__device__ __forceinline__ float xrow_sum(float x) { x = tr16(x, x); return tr32(x, x); }
__device__ __forceinline__ float xrow_max(float x) {
  auto s = __builtin_amdgcn_permlane16_swap(__float_as_uint(x), __float_as_uint(x), false, false);
  x = fmaxf(__uint_as_float(s[0]), __uint_as_float(s[1]));
  auto t = __builtin_amdgcn_permlane32_swap(__float_as_uint(x), __float_as_uint(x), false, false);
  return fmaxf(__uint_as_float(t[0]), __uint_as_float(t[1]));
}
__device__ __forceinline__ float wave_sum(float v) { return xrow_sum(row_sum(v)); }
__device__ __forceinline__ float wave_max(float v) { return xrow_max(row_max(v)); }

#define LDNT(dst, ptr) asm volatile("global_load_dwordx4 %0, %1, off nt" : "=v"(dst) : "v"(ptr) : "memory")
#define WAITVM(n) asm volatile("s_waitcnt vmcnt(" #n ")" ::: "memory")
#define PIN4(a, b, c, d) asm volatile("" : "+v"(a), "+v"(b), "+v"(c), "+v"(d)::"memory")
#define LGKM0() asm volatile("s_waitcnt lgkmcnt(0)" ::: "memory")
template <int N> __device__ __forceinline__ void waitvm() { asm volatile("s_waitcnt vmcnt(%0)" ::"i"(N) : "memory"); }
__device__ __forceinline__ void dma16(const u8* src, u32 lds_dst) {
  u32 keep;
  asm volatile("s_mov_b32 %0, m0\n\ts_mov_b32 m0, %2\n\ts_nop 0\n\tglobal_load_lds_dwordx4 %1, off nt\n\ts_mov_b32 m0, %0"
               : "=&s"(keep) : "v"(src), "s"(lds_dst) : "memory");
}
__device__ __forceinline__ void lds_wait(volatile LAS u32* p, u32 target) {
#pragma unroll 1
  for (u32 spins = 0; spins < SPIN_LDS; ++spins) {
    if (__builtin_amdgcn_readfirstlane(*p) >= target) break;
  }
  asm volatile("" ::: "memory");
}
__device__ __forceinline__ void lds_signal(LAS u32* p, int lane) {
  LGKM0();
  if (lane == 0) __hip_atomic_fetch_add(p, 1u, __ATOMIC_RELAXED, __HIP_MEMORY_SCOPE_WORKGROUP);
}
__device__ __forceinline__ u32 lds_arrive(LAS u32* p, int lane) {
  LGKM0();
  u32 old = 0;
  if (lane == 0) old = __hip_atomic_fetch_add(p, 1u, __ATOMIC_RELAXED, __HIP_MEMORY_SCOPE_WORKGROUP);
  return __builtin_amdgcn_readfirstlane(old);
}
__device__ __forceinline__ v4u ld_sc1(__amdgpu_buffer_rsrc_t rs, u32 off) { return __builtin_amdgcn_raw_buffer_load_b128(rs, (int)off, 0, 16); }
__device__ __forceinline__ void st_sc1(__amdgpu_buffer_rsrc_t rs, u32 off, v4u v) { __builtin_amdgcn_raw_buffer_store_b128(v, rs, (int)off, 0, 16); }
__device__ __forceinline__ u32 bad32(v4u v, u32 t32) { return (v.y ^ t32) | (v.w ^ t32); }
__device__ __forceinline__ u32 bad40(v4u v, u32 t32, u32 t8) { return (v.y ^ t32) | (v.w ^ t32) | (((v.x ^ t8) | (v.z ^ t8)) & 0xffu); }
__device__ __forceinline__ u32 pk24(float v, u32 t8) { return (__float_as_uint(v) & 0xffffff00u) | t8; }
__device__ __forceinline__ float un24(u32 v) { return __uint_as_float(v & 0xffffff00u); }
__device__ __forceinline__ float fdiv(float a, float d, float rd) {
  float q = a * rd;
  return fmaf(fmaf(-q, d, a), rd, q);
}

#define STAMP(id) do {} while (0)

#define PIN2(a, b) asm volatile("" : "+v"(a), "+v"(b)::"memory")
#define PERMPAIR(bt, e, wa, wb) do { PIN2(wa, wb); \
    u32 t_[8]; \
    _Pragma("unroll") for (int d_ = 0; d_ < 4; ++d_) { t_[2 * d_] = plo(wa[d_], wb[d_]); t_[2 * d_ + 1] = phi(wa[d_], wb[d_]); } \
    asm volatile("" : "+v"(t_[0]), "+v"(t_[1]), "+v"(t_[2]), "+v"(t_[3]), "+v"(t_[4]), "+v"(t_[5]), "+v"(t_[6]), "+v"(t_[7])); \
    _Pragma("unroll") for (int d_ = 0; d_ < 8; ++d_) bt[d_][e] = t_[d_]; } while (0)
__device__ __forceinline__ f2 gemv_mfma(const v4u (&bt)[8], v4u xa, int lane) {
  const f4 z = {0.f, 0.f, 0.f, 0.f};
  const bool set = lane & 8;
  float v[8];
#pragma unroll
  for (int t = 0; t < 8; ++t) { const f4 d = mfma(xa, bt[t], z); v[t] = set ? d.y : d.x; }
  const bool k0 = lane & 16, k1 = lane & 32;
  float u0 = k1 ? (k0 ? v[6] : v[4]) : (k0 ? v[2] : v[0]);
  float u1 = k1 ? (k0 ? v[7] : v[5]) : (k0 ? v[3] : v[1]);
  u0 += dpp<ROR8>(u0); u1 += dpp<ROR8>(u1);
  return f2{u0, u1};
}

__global__ void __launch_bounds__(NTHR, 1) fused_decode_kernel(Params p) {
  __shared__ __attribute__((aligned(16))) u8 smem[L_BYTES];
  LAS u8* L = (LAS u8*)smem;
  volatile LAS u32* C = (volatile LAS u32*)(L + L_CTRL);
  LAS u32* CA = (LAS u32*)(L + L_CTRL);
  const int lane = threadIdx.x & 63, wave = __builtin_amdgcn_readfirstlane(threadIdx.x >> 6);
  const int bid = blockIdx.x, xcd = bid & 7, loc = bid >> 3, r = 8 * xcd + (loc >> 2), j = loc & 3;
  const int g = r >> 4, s = (r & 15) * 4 + j;
  const bool has_w1 = (loc >> 2) < 7;
  const int sl = 7 * xcd + (loc >> 2);
  const int ci = lane & 15, kg = lane >> 4;
  const __amdgpu_buffer_rsrc_t rs = __builtin_amdgcn_make_buffer_rsrc((void*)p.ws, 0, WS_END, 0x00020000);

  if (__builtin_expect(wave < 4, 1)) {
    const int w = wave, set = ci >> 3, ch = ci & 7;
    v4u a[8], b[8], bt[8], bo[8];
    {
      const u8* q = p.wq + (size_t)(256 * j + 64 * w + 32 * set + 8 * kg) * 8192 + 128 * r + 16 * ch;
#pragma unroll
      for (int i = 0; i < 8; ++i) LDNT(a[i], q + i * 8192);
    }
    if (w == 0) STAMP(0);
    __builtin_amdgcn_s_barrier();
    const size_t kvoff = ((size_t)g * SK + (SK - WIN) + 64 * s) * 256;
    const u32 lbase = (u32)(uintptr_t)L;
    const u8* kp = p.kc + kvoff + (16 * w + kg) * 256;
    const u8* vp = p.vc + kvoff + w * 4096 + lane * 16;
    const u8* q1 = p.w1 + (size_t)(256 * j + 64 * w + 32 * set + 8 * kg) * 7168 + 128 * sl + 16 * ch;
    const u8* qo = p.wo + (size_t)(64 * r + 32 * set + 8 * kg) * 2048 + 512 * j + 128 * w + 16 * ch;
    const u8* q2 = p.w2 + (size_t)(56 * r + 28 * set + 7 * kg) * 2048 + 512 * j + 128 * w + 16 * ch;
    const u32 fragoff = (32 * (ci & 1) + 8 * kg) * 2;
    u32 t32 = 0, t8 = 0;
#define EV(XX) do { \
      if ((XX) < 8 && ((XX) & 1)) PERMPAIR(bt, ((XX) >> 1) & 3, a[((XX) - 1) & 7], a[(XX) & 7]); \
      if ((XX) == 7) { \
        lds_wait(C + C_LN, 1); \
        const f2 t = gemv_mfma(bt, *(const LAS v4u*)(L + L_XS + 128 * w + fragoff), lane); \
        t32 = __builtin_amdgcn_readfirstlane(C[C_T32]); t8 = __builtin_amdgcn_readfirstlane(C[C_T8]); \
        if (!set) *(LAS f2*)(L + L_QPART + w * 256 + (8 * ch + 2 * kg) * 4) = t; \
        if (lds_arrive(CA + C_QP, lane) == 3) { \
          if (lane < 32) { \
            const LAS f2* pp = (const LAS f2*)(L + L_QPART + lane * 8); \
            const f2 u = (pp[0] + pp[32]) + (pp[64] + pp[96]); \
            st_sc1(rs, WS_QP + (j * 4096 + 64 * r + 2 * lane) * 8, v4u{__float_as_uint(u.x), t32, __float_as_uint(u.y), t32}); \
          } \
          if (lane == 0) C[C_QD] = 1u; \
          STAMP(10); \
        } \
      } \
      if ((XX) == 15 && has_w1) lds_signal(CA + C_KV, lane); \
      if ((XX) >= 16 && (XX) < 24 && ((XX) & 1) && has_w1) PERMPAIR(bt, ((XX) >> 1) & 3, a[((XX) - 1) & 7], a[(XX) & 7]); \
      if ((XX) == 23 && has_w1) { \
        lds_wait(C + C_LY, 1); \
        const f2 t = gemv_mfma(bt, *(const LAS v4u*)(L + L_YS + 128 * w + fragoff), lane); \
        if (!set) *(LAS f2*)(L + L_HPART + w * 256 + (8 * ch + 2 * kg) * 4) = t; \
        if (lds_arrive(CA + C_HP, lane) == 3) { \
          if (lane < 32) { \
            const LAS f2* pp = (const LAS f2*)(L + L_HPART + lane * 8); \
            const f2 u = (pp[0] + pp[32]) + (pp[64] + pp[96]); \
            st_sc1(rs, WS_HP + (j * 3584 + 64 * sl + 2 * lane) * 8, v4u{__float_as_uint(u.x), t32, __float_as_uint(u.y), t32}); \
          } \
          if (lane == 0) C[C_HD] = 1u; \
          STAMP(11); \
        } \
      } \
      if ((XX) >= 24 && (XX) < 32 && ((XX) & 1)) PERMPAIR(bo, ((XX) >> 1) & 3, b[((XX) - 1) & 7], b[(XX) & 7]); \
    } while (0)
#define EVS(n) do { if ((n) == 8) { _Pragma("unroll") for (int y_ = 0; y_ <= 8 - PW; ++y_) EV(y_); } else EV((n) - PW); } while (0)
#pragma unroll
    for (int i = 0; i < 4; ++i) { waitvm<PW - 1>(); dma16(kp + i * 1024 + ((ci ^ ((4 * i + kg) & 15)) << 4), lbase + L_K + w * 4096 + i * 1024); EVS(8 + i); }
#pragma unroll
    for (int i = 0; i < 4; ++i) { waitvm<PW - 1>(); dma16(vp + i * 1024, lbase + L_V + w * 4096 + i * 1024); EVS(12 + i); }
    if (has_w1) {
#pragma unroll
      for (int i = 0; i < 8; ++i) { waitvm<PW - 1>(); LDNT(a[i], q1 + i * 7168); EVS(16 + i); }
    } else {
      waitvm<0>();
      lds_signal(CA + C_KV, lane);
    }
#pragma unroll
    for (int i = 0; i < 8; ++i) { waitvm<PW - 1>(); LDNT(b[i], qo + i * 2048); EVS(24 + i); }
#pragma unroll
    for (int i = 0; i < 7; ++i) { waitvm<PW - 1>(); LDNT(a[i], q2 + i * 2048); EVS(32 + i); }
    if (w == 0) STAMP(1);
    {
      lds_wait(C + C_O, 4);
      const f2 t = gemv_mfma(bo, *(const LAS v4u*)(L + L_OIMG + fragoff), lane);
      if (!set) st_sc1(rs, WS_FP + (r * 1024 + 256 * j + 64 * w + 8 * ch + 2 * kg) * 8, v4u{pk24(t.x, t8), t32, pk24(t.y, t8), t32});
      if (w == 0) STAMP(7);
    }
    {
      waitvm<0>();
      if (w == 0) STAMP(2);
      a[7] = v4u{0u, 0u, 0u, 0u};
      PERMPAIR(bt, 0, a[0], a[1]); PERMPAIR(bt, 1, a[2], a[3]); PERMPAIR(bt, 2, a[4], a[5]); PERMPAIR(bt, 3, a[6], a[7]);
      lds_wait(C + C_H, 1);
      const f2 t = gemv_mfma(bt, *(const LAS v4u*)(L + L_HIMG + fragoff), lane);
      if (!set) st_sc1(rs, WS_FP + ((64 + r) * 1024 + 256 * j + 64 * w + 8 * ch + 2 * kg) * 8, v4u{pk24(t.x, t8), t32, pk24(t.y, t8), t32});
      if (lane == 0) __hip_atomic_fetch_add(CA + C_FB, 1u, __ATOMIC_RELAXED, __HIP_MEMORY_SCOPE_WORKGROUP);
      if (w == 0) STAMP(13);
    }
  } else if (wave < 8) {
    const int a = wave - 4;
    __builtin_amdgcn_s_barrier();
    lds_wait(C + C_QD, 1);
    const u32 t32 = __builtin_amdgcn_readfirstlane(C[C_T32]), t8 = __builtin_amdgcn_readfirstlane(C[C_T8]);
    {
      v4u v[8];
      const u32 o0 = WS_QP + (1024 * g + 256 * a + 2 * lane) * 8;
      u32 spins = 0, bad;
#pragma unroll 1
      do {
#pragma unroll
        for (int k = 0; k < 8; ++k) v[k] = ld_sc1(rs, o0 + (k >> 1) * 32768 + (k & 1) * 1024);
        bad = 0;
#pragma unroll
        for (int k = 0; k < 8; ++k) bad |= bad32(v[k], t32);
        asm volatile("" ::: "memory");
      } while (bad != 0 && ++spins < SPIN_GLB);
#pragma unroll
      for (int h = 0; h < 2; ++h) {
        const float q0 = (__uint_as_float(v[h].x) + __uint_as_float(v[2 + h].x)) + (__uint_as_float(v[4 + h].x) + __uint_as_float(v[6 + h].x));
        const float q1 = (__uint_as_float(v[h].z) + __uint_as_float(v[2 + h].z)) + (__uint_as_float(v[4 + h].z) + __uint_as_float(v[6 + h].z));
        *(LAS u32*)(L + L_QIMG + (2 * a + h) * 272 + lane * 4) = pkbf(q0, q1);
      }
      lds_signal(CA + C_Q, lane);
    }
    if (a == 0) STAMP(3);
    lds_wait(C + C_KV, 4);
    v4u kf[4], vf[2][2];
#pragma unroll
    for (int t = 0; t < 4; ++t) kf[t] = *(const LAS v4u*)(L + L_K + (16 * a + ci) * 256 + (((4 * t + kg) ^ ci) << 4));
#pragma unroll
    for (int u = 0; u < 2; ++u) {
      u32 vv[8];
#pragma unroll
      for (int e = 0; e < 8; ++e) vv[e] = *(const LAS u32*)(L + L_V + (32 * u + 8 * kg + e) * 256 + 64 * a + 4 * ci);
#pragma unroll
      for (int e = 0; e < 4; ++e) { vf[u][0][e] = plo(vv[2 * e], vv[2 * e + 1]); vf[u][1][e] = phi(vv[2 * e], vv[2 * e + 1]); }
    }
    PIN4(vf[0][0], vf[0][1], vf[1][0], vf[1][1]);
    lds_wait(C + C_Q, 4);
    if (a == 0) STAMP(4);
    constexpr float LOG2E = 1.4426950408889634f;
    {
      f4 sc = {0.f, 0.f, 0.f, 0.f};
#pragma unroll
      for (int t = 0; t < 4; ++t) {
        const v4u qf = *(const LAS v4u*)(L + L_QIMG + (ci & 7) * 272 + 64 * t + 16 * kg);
        sc = mfma(kf[t], qf, sc);
      }
#pragma unroll
      for (int e = 0; e < 4; ++e) sc[e] = rbf(rbf(sc[e]) * 0.08838834764831845f);
      const float m = xrow_max(fmaxf(fmaxf(sc.x, sc.y), fmaxf(sc.z, sc.w)));
      f4 pe;
#pragma unroll
      for (int e = 0; e < 4; ++e) pe[e] = __builtin_amdgcn_exp2f((sc[e] - m) * LOG2E);
      if (ci < 8) *(LAS f4*)(L + L_SC + ci * 272 + (16 * a + 4 * kg) * 4) = pe;
      const float l = xrow_sum((pe.x + pe.y) + (pe.z + pe.w));
      if (lane < 8) *(LAS f2*)(L + L_MLX + lane * 32 + a * 8) = f2{m, l};
      lds_signal(CA + C_S, lane);
    }
    lds_wait(C + C_S, 4);
    {
      const f4 ml01 = *(const LAS f4*)(L + L_MLX + (ci & 7) * 32), ml23 = *(const LAS f4*)(L + L_MLX + (ci & 7) * 32 + 16);
      f4 sv[4];
#pragma unroll
      for (int e = 0; e < 4; ++e) sv[e] = *(const LAS f4*)(L + L_SC + (ci & 7) * 272 + (32 * (e >> 1) + 8 * kg + 4 * (e & 1)) * 4);
      const float m = fmaxf(fmaxf(ml01.x, ml01.z), fmaxf(ml23.x, ml23.z));
      const float f0 = __builtin_amdgcn_exp2f((ml01.x - m) * LOG2E), f1 = __builtin_amdgcn_exp2f((ml01.z - m) * LOG2E);
      const float f2_ = __builtin_amdgcn_exp2f((ml23.x - m) * LOG2E), f3 = __builtin_amdgcn_exp2f((ml23.z - m) * LOG2E);
      const float lsum = (f0 * ml01.y + f1 * ml01.w) + (f2_ * ml23.y + f3 * ml23.w);
      const float fa = (kg & 2) ? f1 : f0, fb = (kg & 2) ? f3 : f2_;
      v4u pf[2];
#pragma unroll
      for (int e = 0; e < 4; ++e) {
        const f4 pe = sv[e] * ((e >> 1) ? fb : fa);
        pf[e >> 1][2 * (e & 1)] = pkbf(pe.x, pe.y);
        pf[e >> 1][2 * (e & 1) + 1] = pkbf(pe.z, pe.w);
      }
      f4 o0 = {0.f, 0.f, 0.f, 0.f}, o1 = {0.f, 0.f, 0.f, 0.f};
      o0 = mfma(pf[0], vf[0][0], o0); o1 = mfma(pf[0], vf[0][1], o1);
      o0 = mfma(pf[1], vf[1][0], o0); o1 = mfma(pf[1], vf[1][1], o1);
      const int hb = 4 * (kg & 1) + 2 * (kg >> 1);
      const bool up = kg >= 2;
      const u32 ob = WS_OP + ((((8 * g + hb) * 2 + (a >> 1)) * 64 + s) * 64 + 32 * (a & 1) + 2 * ci) * 8;
      st_sc1(rs, ob, v4u{pk24(up ? o0.z : o0.x, t8), t32, pk24(up ? o1.z : o1.x, t8), t32});
      st_sc1(rs, ob + 2 * 64 * 64 * 8, v4u{pk24(up ? o0.w : o0.y, t8), t32, pk24(up ? o1.w : o1.y, t8), t32});
      if (a == 0 && lane < 8) st_sc1(rs, WS_ML + ((8 * g + lane) * 64 + s) * 16, v4u{pk24(m, t8), t32, pk24(lsum, t8), t32});
    }
    if (a == 0) STAMP(5);
    {
      v4u v[8], ml;
      const int sg = lane >> 3;
      const u32 o0 = WS_OP + ((r * 64 + sg) * 64 + 16 * a + 2 * (lane & 7)) * 8;
      const u32 om = WS_ML + ((r >> 1) * 64 + lane) * 16;
      u32 spins = 0, bad;
#pragma unroll 1
      do {
        ml = ld_sc1(rs, om);
#pragma unroll
        for (int k = 0; k < 8; ++k) v[k] = ld_sc1(rs, o0 + k * 4096);
        bad = bad40(ml, t32, t8);
#pragma unroll
        for (int k = 0; k < 8; ++k) bad |= bad40(v[k], t32, t8);
        asm volatile("" ::: "memory");
      } while (bad != 0 && ++spins < SPIN_GLB);
      const float ms = un24(ml.x), ls = un24(ml.z);
      const float M = wave_max(ms);
      const float ws_ = __builtin_amdgcn_exp2f((ms - M) * LOG2E);
      const float inv = __builtin_amdgcn_rcpf(wave_sum(ws_ * ls));
      float e0 = 0.f, e1 = 0.f;
#pragma unroll
      for (int k = 0; k < 8; ++k) {
        const float fk = __shfl(ws_, 8 * k + sg);
        e0 = fmaf(fk, un24(v[k].x), e0); e1 = fmaf(fk, un24(v[k].z), e1);
      }
      float e = tr32(e0, e1);
      e = tr16(e, e);
      e += dpp<ROR8>(e);
      e *= inv;
      if ((lane & 24) == 0) *(LAS unsigned short*)(L + L_OIMG + (16 * a + 2 * (lane & 7) + (lane >> 5)) * 2) = (unsigned short)pkbf(e, e);
      lds_signal(CA + C_O, lane);
    }
    if (a == 0) STAMP(6);
  } else {
    v2u xr[4];
#pragma unroll
    for (int q = 0; q < 4; ++q) xr[q] = *(const GAS v2u*)(p.x + (j ^ q) * 512 + lane * 8);
    const u64 eold = __hip_atomic_load((GAS u64*)(p.ws + WS_EPOCH), __ATOMIC_RELAXED, __HIP_MEMORY_SCOPE_AGENT);
    const int li = (lane & 31) < 28 ? (lane & 31) : 27;
    const u32 b1w = *(const GAS u32*)(p.b1 + (56 * r + 2 * li) * 2);
    const int cg = 32 * xcd + loc;
    const u32 xfin = *(const GAS unsigned short*)(p.x + (4 * cg + kg) * 2), bfin = *(const GAS unsigned short*)(p.b2 + (4 * cg + kg) * 2);
    if (lane < 16) C[lane] = 0u;
    if (lane < 32) *(LAS u32*)(L + L_HIMG + lane * 4) = 0u;
    LGKM0();
    __builtin_amdgcn_s_barrier();
    __builtin_amdgcn_sched_barrier(0);
    f4 v[4];
#pragma unroll
    for (int q = 0; q < 4; ++q)
      v[q] = f4{bf2f(xr[q].x & 0xffffu), __uint_as_float(xr[q].x & 0xffff0000u), bf2f(xr[q].y & 0xffffu), __uint_as_float(xr[q].y & 0xffff0000u)};
    const u64 e = eold + 1;
    const u32 t32 = __builtin_amdgcn_readfirstlane((u32)e), ehi = __builtin_amdgcn_readfirstlane((u32)(e >> 32)), t8 = ehi & 0xffu;
    if (lane == 0) { C[C_T32] = t32; C[C_T8] = t8; }
#pragma unroll
    for (int pass = 0; pass < 2; ++pass) {
      const f4 s4 = (v[0] + v[1]) + (v[2] + v[3]);
      const float mean = wave_sum((s4.x + s4.y) + (s4.z + s4.w)) * (1.f / 1024.f);
      f4 d[4];
#pragma unroll
      for (int q = 0; q < 4; ++q) { d[q] = v[q] - mean; d[q] *= d[q]; }
      const f4 q4 = (d[0] + d[1]) + (d[2] + d[3]);
      const float var = wave_sum((q4.x + q4.y) + (q4.z + q4.w)) * (1.f / 1024.f);
      const float mb = rbf(mean), den = rbf(__builtin_sqrtf(rbf(rbf(var) + 1e-5f))), rd = __builtin_amdgcn_rcpf(den);
#pragma unroll
      for (int z = 0; z < 4; ++z) v[0][z] = fdiv(rbf(v[0][z] - mb), den, rd);
      const v2u pk = {pkbf(v[0].x, v[0].y), pkbf(v[0].z, v[0].w)};
      *(LAS v2u*)(L + (pass ? L_YS : L_XS) + lane * 8) = pk;
      lds_signal(CA + (pass ? C_LY : C_LN), lane);
      if (pass == 0) {
        STAMP(9);
        __builtin_amdgcn_sched_barrier(0);
        v[0] = f4{bf2f(pk.x & 0xffffu), __uint_as_float(pk.x & 0xffff0000u), bf2f(pk.y & 0xffffu), __uint_as_float(pk.y & 0xffff0000u)};
#pragma unroll
        for (int q = 1; q < 4; ++q)
#pragma unroll
          for (int z = 0; z < 4; ++z) v[q][z] = rbf(fdiv(rbf(v[q][z] - mb), den, rd));
      }
    }
    if (has_w1) lds_wait(C + C_HD, 1); else lds_wait(C + C_KV, 4);
    {
      const u32 o0 = WS_HP + ((lane >> 5) * 3584 + 56 * r + 2 * li) * 8;
      v4u v0, v1;
      u32 spins = 0, bad;
#pragma unroll 1
      do {
        v0 = ld_sc1(rs, o0); v1 = ld_sc1(rs, o0 + 2 * 3584 * 8);
        bad = bad32(v0, t32) | bad32(v1, t32);
        asm volatile("" ::: "memory");
      } while (bad != 0 && ++spins < SPIN_GLB);
      float h0 = __uint_as_float(v0.x) + __uint_as_float(v1.x), h1 = __uint_as_float(v0.z) + __uint_as_float(v1.z);
      h0 = tr32(h0, h0); h1 = tr32(h1, h1);
      float hh[2] = {rbf(rbf(h0) + bf2f(b1w & 0xffffu)), rbf(rbf(h1) + __uint_as_float(b1w & 0xffff0000u))};
#pragma unroll
      for (int z = 0; z < 2; ++z) {
        const float t = hh[z], u = 0.7978845608028654f * (t + 0.044715f * t * t * t);
        const float th = 1.f - 2.f / (__expf(2.f * u) + 1.f);
        const int n = 2 * lane + z, st = n >= 28, nn = n - 28 * st, k7 = nn / 7;
        if (lane < 28) *(LAS unsigned short*)(L + L_HIMG + (32 * st + 8 * k7 + (nn - 7 * k7)) * 2) = (unsigned short)pkbf(0.5f * t * (1.f + th), 0.f);
      }
      lds_signal(CA + C_H, lane);
    }
    STAMP(12);
    {
      v4u v[4];
      const u32 o0 = WS_FP + (lane * 1024 + 4 * cg) * 8;
      lds_wait(C + C_FB, 4);
      u32 spins = 0, bad;
#pragma unroll 1
      do {
        v[0] = ld_sc1(rs, o0); v[1] = ld_sc1(rs, o0 + 16);
        v[2] = ld_sc1(rs, o0 + 64 * 1024 * 8); v[3] = ld_sc1(rs, o0 + 64 * 1024 * 8 + 16);
        bad = bad40(v[0], t32, t8) | bad40(v[1], t32, t8) | bad40(v[2], t32, t8) | bad40(v[3], t32, t8);
        asm volatile("" ::: "memory");
      } while (bad != 0 && ++spins < SPIN_GLB);
      float A = tr16(tr32(un24(v[0].x), un24(v[1].x)), tr32(un24(v[0].z), un24(v[1].z)));
      float B = tr16(tr32(un24(v[2].x), un24(v[3].x)), tr32(un24(v[2].z), un24(v[3].z)));
      A = row_sum(A); B = row_sum(B);
      const float res = rbf(bf2f(xfin) + rbf(A)) + rbf(rbf(B) + bf2f(bfin));
      if (ci == 0) *(GAS unsigned short*)(p.out + (4 * cg + kg) * 2) = (unsigned short)(pkbf(res, res) & 0xffffu);
      if (bid == 0 && lane == 0)
        __hip_atomic_store((GAS u64*)(p.ws + WS_EPOCH), e, __ATOMIC_RELAXED, __HIP_MEMORY_SCOPE_AGENT);
      STAMP(8);
    }
  }
}
}

extern "C" void kernel_launch(void* const* d_in, const int* in_sizes, int n_in, void* d_out, int out_size, void* d_ws, size_t ws_size,
                              hipStream_t stream) {
  Params p;
  p.x = (const u8*)d_in[0]; p.kc = (const u8*)d_in[1]; p.vc = (const u8*)d_in[2]; p.wq = (const u8*)d_in[3]; p.wo = (const u8*)d_in[4];
  p.w1 = (const u8*)d_in[6]; p.b1 = (const u8*)d_in[7]; p.w2 = (const u8*)d_in[8]; p.b2 = (const u8*)d_in[9];
  p.out = (u8*)d_out; p.ws = (u8*)d_ws;
  hipLaunchKernelGGL(fused_decode_kernel, dim3(256), dim3(NTHR), 0, stream, p);
}
```

```cpp
#include <hip/hip_runtime.h>
#include <stdint.h>

#define GAS __attribute__((address_space(1)))
#define LAS __attribute__((address_space(3)))
typedef unsigned u32;
typedef unsigned long long u64;
typedef unsigned char u8;
typedef u32 v4u __attribute__((ext_vector_type(4)));
typedef u32 v2u __attribute__((ext_vector_type(2)));
typedef float f4 __attribute__((ext_vector_type(4)));
typedef float f2 __attribute__((ext_vector_type(2)));
typedef short bf16x8 __attribute__((ext_vector_type(8)));
typedef __bf16 bf16x2 __attribute__((ext_vector_type(2)));

#define STAMPS 1
#define PW 6

namespace {
constexpr int SK = 65536, WIN = 4096;
constexpr int NWAVES = 9, NTHR = NWAVES * 64;
constexpr u32 WS_EPOCH = 0;
constexpr u32 WS_QP = 4096;
constexpr u32 WS_HP = WS_QP + 4 * 4096 * 8;
constexpr u32 WS_ML = WS_HP + 4 * 3584 * 8;
constexpr u32 WS_OP = WS_ML + 32 * 64 * 16;
constexpr u32 WS_FP = WS_OP + 32 * 2 * 64 * 64 * 8;
constexpr u32 WS_END = WS_FP + 2 * 64 * 1024 * 8;
constexpr u32 WS_STAMP = 8u << 20;
constexpr int L_K = 0, L_V = 16384, L_XS = 32768, L_YS = L_XS + 512, L_QPART = L_YS + 512, L_HPART = L_QPART + 1024,
              L_QIMG = L_HPART + 1024, L_SC = L_QIMG + 2304, L_MLX = L_SC + 2304, L_OIMG = L_MLX + 256, L_HIMG = L_OIMG + 128,
              L_CTRL = L_HIMG + 128, L_USED = L_CTRL + 256, L_BYTES = 90112;
static_assert(L_USED <= L_BYTES, "lds");
enum { C_LN = 0, C_LY, C_QP, C_QD, C_KV, C_HP, C_HD, C_Q, C_S, C_O, C_H, C_FB, C_T32, C_T8, C_EHI };
constexpr u32 SPIN_LDS = 1u << 22, SPIN_GLB = 1u << 18;

struct Params { const u8 *x, *kc, *vc, *wq, *wo, *w1, *b1, *w2, *b2; u8 *out, *ws; };

__device__ __forceinline__ float bf2f(u32 b) { return __uint_as_float(b << 16); }
__device__ __forceinline__ u32 pkbf(float lo, float hi) {
  bf16x2 v = {(__bf16)lo, (__bf16)hi};
  return __builtin_bit_cast(u32, v);
}
__device__ __forceinline__ float rbf(float f) { return (float)(__bf16)f; }
__device__ __forceinline__ float dot2(u32 a, u32 b, float c) {
  return __builtin_amdgcn_fdot2_f32_bf16(__builtin_bit_cast(bf16x2, a), __builtin_bit_cast(bf16x2, b), c, false);
}
__device__ __forceinline__ u32 plo(u32 a, u32 b) { return __builtin_amdgcn_perm(b, a, 0x05040100u); }
__device__ __forceinline__ u32 phi(u32 a, u32 b) { return __builtin_amdgcn_perm(b, a, 0x07060302u); }
__device__ __forceinline__ f4 mfma(v4u a, v4u b, f4 c) {
  return __builtin_amdgcn_mfma_f32_16x16x32_bf16(__builtin_bit_cast(bf16x8, a), __builtin_bit_cast(bf16x8, b), c, 0, 0, 0);
}
template <int CTRL> __device__ __forceinline__ float dpp(float x) {
  return __builtin_bit_cast(float, __builtin_amdgcn_mov_dpp(__builtin_bit_cast(int, x), CTRL, 0xf, 0xf, true));
}
constexpr int XOR1 = 0xB1, XOR2 = 0x4E, HMIR = 0x141, ROR8 = 0x128, ROR4 = 0x124;
__device__ __forceinline__ float tr32(float a, float b) {
  auto s = __builtin_amdgcn_permlane32_swap(__float_as_uint(a), __float_as_uint(b), false, false);
  return __uint_as_float(s[0]) + __uint_as_float(s[1]);
}
__device__ __forceinline__ float tr16(float a, float b) {
  auto s = __builtin_amdgcn_permlane16_swap(__float_as_uint(a), __float_as_uint(b), false, false);
  return __uint_as_float(s[0]) + __uint_as_float(s[1]);
}
__device__ __forceinline__ float row_sum(float v) {
  v += dpp<XOR1>(v); v += dpp<XOR2>(v); v += dpp<HMIR>(v); v += dpp<ROR8>(v);
  return v;
}
__device__ __forceinline__ float row_max(float v) {
  v = fmaxf(v, dpp<XOR1>(v)); v = fmaxf(v, dpp<XOR2>(v)); v = fmaxf(v, dpp<HMIR>(v)); v = fmaxf(v, dpp<ROR8>(v));
  return v;
}
__device__ __forceinline__ float xrow_sum(float x) { x = tr16(x, x); return tr32(x, x); }
__device__ __forceinline__ float xrow_max(float x) {
  auto s = __builtin_amdgcn_permlane16_swap(__float_as_uint(x), __float_as_uint(x), false, false);
  x = fmaxf(__uint_as_float(s[0]), __uint_as_float(s[1]));
  auto t = __builtin_amdgcn_permlane32_swap(__float_as_uint(x), __float_as_uint(x), false, false);
  return fmaxf(__uint_as_float(t[0]), __uint_as_float(t[1]));
}
__device__ __forceinline__ float wave_sum(float v) { return xrow_sum(row_sum(v)); }
__device__ __forceinline__ float wave_max(float v) { return xrow_max(row_max(v)); }

#define LDNT(dst, ptr) asm volatile("global_load_dwordx4 %0, %1, off nt" : "=v"(dst) : "v"(ptr) : "memory")
#define WAITVM(n) asm volatile("s_waitcnt vmcnt(" #n ")" ::: "memory")
#define PIN4(a, b, c, d) asm volatile("" : "+v"(a), "+v"(b), "+v"(c), "+v"(d)::"memory")
#define LGKM0() asm volatile("s_waitcnt lgkmcnt(0)" ::: "memory")
template <int N> __device__ __forceinline__ void waitvm() { asm volatile("s_waitcnt vmcnt(%0)" ::"i"(N) : "memory"); }
__device__ __forceinline__ void dma16(const u8* src, u32 lds_dst) {
  u32 keep;
  asm volatile("s_mov_b32 %0, m0\n\ts_mov_b32 m0, %2\n\ts_nop 0\n\tglobal_load_lds_dwordx4 %1, off nt\n\ts_mov_b32 m0, %0"
               : "=&s"(keep) : "v"(src), "s"(lds_dst) : "memory");
}
__device__ __forceinline__ void lds_wait(volatile LAS u32* p, u32 target) {
#pragma unroll 1
  for (u32 spins = 0; spins < SPIN_LDS; ++spins) {
    if (__builtin_amdgcn_readfirstlane(*p) >= target) break;
  }
  asm volatile("" ::: "memory");
}
__device__ __forceinline__ void lds_signal(LAS u32* p, int lane) {
  LGKM0();
  if (lane == 0) __hip_atomic_fetch_add(p, 1u, __ATOMIC_RELAXED, __HIP_MEMORY_SCOPE_WORKGROUP);
}
__device__ __forceinline__ u32 lds_arrive(LAS u32* p, int lane) {
  LGKM0();
  u32 old = 0;
  if (lane == 0) old = __hip_atomic_fetch_add(p, 1u, __ATOMIC_RELAXED, __HIP_MEMORY_SCOPE_WORKGROUP);
  return __builtin_amdgcn_readfirstlane(old);
}
__device__ __forceinline__ v4u ld_sc1(__amdgpu_buffer_rsrc_t rs, u32 off) { return __builtin_amdgcn_raw_buffer_load_b128(rs, (int)off, 0, 16); }
__device__ __forceinline__ void st_sc1(__amdgpu_buffer_rsrc_t rs, u32 off, v4u v) { __builtin_amdgcn_raw_buffer_store_b128(v, rs, (int)off, 0, 16); }
__device__ __forceinline__ u32 bad32(v4u v, u32 t32) { return (v.y ^ t32) | (v.w ^ t32); }
__device__ __forceinline__ u32 bad40(v4u v, u32 t32, u32 t8) { return (v.y ^ t32) | (v.w ^ t32) | (((v.x ^ t8) | (v.z ^ t8)) & 0xffu); }
__device__ __forceinline__ u32 pk24(float v, u32 t8) { return (__float_as_uint(v) & 0xffffff00u) | t8; }
__device__ __forceinline__ float un24(u32 v) { return __uint_as_float(v & 0xffffff00u); }
__device__ __forceinline__ float fdiv(float a, float d, float rd) {
  float q = a * rd;
  return fmaf(fmaf(-q, d, a), rd, q);
}

#define STAMP(id) do { if (lane == 0) ((volatile LAS u64*)(L + L_USED))[id] = __builtin_amdgcn_s_memrealtime(); } while (0)
#define STAMP_DUMP() do { LGKM0(); if (lane == 0 && (blockIdx.x % 16) == 5) { volatile LAS u64* S_ = (volatile LAS u64*)(L + L_USED); u64 b_ = S_[0] & ~0xffffull; \
  printf("bid %3d xcc %d: %d | %d %d %d %d %d %d %d %d %d %d %d %d %d %d %d %d %d %d\n", blockIdx.x, (int)(__builtin_amdgcn_s_getreg(20 | (3 << 11)) & 0xf), (int)(S_[0]-b_), (int)(S_[1]-S_[0]), (int)(S_[2]-S_[0]), (int)(S_[3]-S_[0]), (int)(S_[4]-S_[0]), (int)(S_[5]-S_[0]), (int)(S_[6]-S_[0]), (int)(S_[7]-S_[0]), (int)(S_[8]-S_[0]), (int)(S_[9]-S_[0]), (int)(S_[10]-S_[0]), (int)(S_[11]-S_[0]), (int)(S_[12]-S_[0]), (int)(S_[13]-S_[0]), (int)(S_[14]-S_[0]), (int)(S_[15]-S_[0]), (int)(S_[16]-S_[0]), (int)(S_[17]-S_[0]), (int)(S_[18]-S_[0])); } } while (0)

#define PIN2(a, b) asm volatile("" : "+v"(a), "+v"(b)::"memory")
#define PERMPAIR(bt, e, wa, wb) do { PIN2(wa, wb); \
    u32 t_[8]; \
    _Pragma("unroll") for (int d_ = 0; d_ < 4; ++d_) { t_[2 * d_] = plo(wa[d_], wb[d_]); t_[2 * d_ + 1] = phi(wa[d_], wb[d_]); } \
    asm volatile("" : "+v"(t_[0]), "+v"(t_[1]), "+v"(t_[2]), "+v"(t_[3]), "+v"(t_[4]), "+v"(t_[5]), "+v"(t_[6]), "+v"(t_[7])); \
    _Pragma("unroll") for (int d_ = 0; d_ < 8; ++d_) bt[d_][e] = t_[d_]; } while (0)
__device__ __forceinline__ f2 gemv_mfma(const v4u (&bt)[8], v4u xa, int lane) {
  const f4 z = {0.f, 0.f, 0.f, 0.f};
  const bool set = lane & 8;
  float v[8];
#pragma unroll
  for (int t = 0; t < 8; ++t) { const f4 d = mfma(xa, bt[t], z); v[t] = set ? d.y : d.x; }
  const bool k0 = lane & 16, k1 = lane & 32;
  float u0 = k1 ? (k0 ? v[6] : v[4]) : (k0 ? v[2] : v[0]);
  float u1 = k1 ? (k0 ? v[7] : v[5]) : (k0 ? v[3] : v[1]);
  u0 += dpp<ROR8>(u0); u1 += dpp<ROR8>(u1);
  return f2{u0, u1};
}

__global__ void __launch_bounds__(NTHR, 1) fused_decode_kernel(Params p) {
  __shared__ __attribute__((aligned(16))) u8 smem[L_BYTES];
  LAS u8* L = (LAS u8*)smem;
  volatile LAS u32* C = (volatile LAS u32*)(L + L_CTRL);
  LAS u32* CA = (LAS u32*)(L + L_CTRL);
  const int lane = threadIdx.x & 63, wave = __builtin_amdgcn_readfirstlane(threadIdx.x >> 6);
  const int bid = blockIdx.x, xcd = bid & 7, loc = bid >> 3, r = 8 * xcd + (loc >> 2), j = loc & 3;
  const int g = r >> 4, s = (r & 15) * 4 + j;
  const bool has_w1 = (loc >> 2) < 7;
  const int sl = 7 * xcd + (loc >> 2);
  const int ci = lane & 15, kg = lane >> 4;
  const __amdgpu_buffer_rsrc_t rs = __builtin_amdgcn_make_buffer_rsrc((void*)p.ws, 0, WS_END, 0x00020000);

  if (__builtin_expect(wave < 4, 1)) {
    const int w = wave, set = ci >> 3, ch = ci & 7;
    v4u a[8], b[8], bt[8], bo[8];
    {
      const u8* q = p.wq + (size_t)(256 * j + 64 * w + 32 * set + 8 * kg) * 8192 + 128 * r + 16 * ch;
#pragma unroll
      for (int i = 0; i < 8; ++i) LDNT(a[i], q + i * 8192);
    }
    const u64 tstart = __builtin_amdgcn_s_memrealtime();
    __builtin_amdgcn_s_barrier();
    if (w == 0 && lane == 0) ((volatile LAS u64*)(L + L_USED))[0] = tstart;
    const size_t kvoff = ((size_t)g * SK + (SK - WIN) + 64 * s) * 256;
    const u32 lbase = (u32)(uintptr_t)L;
    const u8* kp = p.kc + kvoff + (16 * w + kg) * 256;
    const u8* vp = p.vc + kvoff + w * 4096 + lane * 16;
    const u8* q1 = p.w1 + (size_t)(256 * j + 64 * w + 32 * set + 8 * kg) * 7168 + 128 * sl + 16 * ch;
    const u8* qo = p.wo + (size_t)(64 * r + 32 * set + 8 * kg) * 2048 + 512 * j + 128 * w + 16 * ch;
    const u8* q2 = p.w2 + (size_t)(56 * r + 28 * set + 7 * kg) * 2048 + 512 * j + 128 * w + 16 * ch;
    const u32 fragoff = (32 * (ci & 1) + 8 * kg) * 2;
    u32 t32 = 0, t8 = 0;
#define EV(XX) do { \
      if ((XX) < 8 && ((XX) & 1)) PERMPAIR(bt, ((XX) >> 1) & 3, a[((XX) - 1) & 7], a[(XX) & 7]); \
      if ((XX) == 7) { \
        lds_wait(C + C_LN, 1); \
        const f2 t = gemv_mfma(bt, *(const LAS v4u*)(L + L_XS + 128 * w + fragoff), lane); \
        t32 = __builtin_amdgcn_readfirstlane(C[C_T32]); t8 = __builtin_amdgcn_readfirstlane(C[C_T8]); \
        if (!set) *(LAS f2*)(L + L_QPART + w * 256 + (8 * ch + 2 * kg) * 4) = t; \
        if (lds_arrive(CA + C_QP, lane) == 3) { \
          if (lane < 32) { \
            const LAS f2* pp = (const LAS f2*)(L + L_QPART + lane * 8); \
            const f2 u = (pp[0] + pp[32]) + (pp[64] + pp[96]); \
            st_sc1(rs, WS_QP + (j * 4096 + 64 * r + 2 * lane) * 8, v4u{__float_as_uint(u.x), t32, __float_as_uint(u.y), t32}); \
          } \
          if (lane == 0) C[C_QD] = 1u; \
          STAMP(10); \
        } \
      } \
      if ((XX) == 15 && has_w1) lds_signal(CA + C_KV, lane); \
      if ((XX) >= 16 && (XX) < 24 && ((XX) & 1) && has_w1) PERMPAIR(bt, ((XX) >> 1) & 3, a[((XX) - 1) & 7], a[(XX) & 7]); \
      if ((XX) == 23 && has_w1) { \
        lds_wait(C + C_LY, 1); \
        const f2 t = gemv_mfma(bt, *(const LAS v4u*)(L + L_YS + 128 * w + fragoff), lane); \
        if (!set) *(LAS f2*)(L + L_HPART + w * 256 + (8 * ch + 2 * kg) * 4) = t; \
        if (lds_arrive(CA + C_HP, lane) == 3) { \
          if (lane < 32) { \
            const LAS f2* pp = (const LAS f2*)(L + L_HPART + lane * 8); \
            const f2 u = (pp[0] + pp[32]) + (pp[64] + pp[96]); \
            st_sc1(rs, WS_HP + (j * 3584 + 64 * sl + 2 * lane) * 8, v4u{__float_as_uint(u.x), t32, __float_as_uint(u.y), t32}); \
          } \
          if (lane == 0) C[C_HD] = 1u; \
          STAMP(11); \
        } \
      } \
      if ((XX) >= 24 && (XX) < 32 && ((XX) & 1)) PERMPAIR(bo, ((XX) >> 1) & 3, b[((XX) - 1) & 7], b[(XX) & 7]); \
    } while (0)
#define EVS(n) do { if ((n) == 8) { _Pragma("unroll") for (int y_ = 0; y_ <= 8 - PW; ++y_) EV(y_); } else EV((n) - PW); } while (0)
#pragma unroll
    for (int i = 0; i < 4; ++i) { waitvm<PW - 1>(); dma16(kp + i * 1024 + ((ci ^ ((4 * i + kg) & 15)) << 4), lbase + L_K + w * 4096 + i * 1024); EVS(8 + i); }
#pragma unroll
    for (int i = 0; i < 4; ++i) { waitvm<PW - 1>(); dma16(vp + i * 1024, lbase + L_V + w * 4096 + i * 1024); EVS(12 + i); }
    if (has_w1) {
#pragma unroll
      for (int i = 0; i < 8; ++i) { waitvm<PW - 1>(); LDNT(a[i], q1 + i * 7168); EVS(16 + i); }
    } else {
      waitvm<0>();
      lds_signal(CA + C_KV, lane);
    }
#pragma unroll
    for (int i = 0; i < 8; ++i) { waitvm<PW - 1>(); LDNT(b[i], qo + i * 2048); EVS(24 + i); }
#pragma unroll
    for (int i = 0; i < 7; ++i) { waitvm<PW - 1>(); LDNT(a[i], q2 + i * 2048); EVS(32 + i); }
    if (w == 0) STAMP(1);
    {
      lds_wait(C + C_O, 4);
      const f2 t = gemv_mfma(bo, *(const LAS v4u*)(L + L_OIMG + fragoff), lane);
      if (!set) st_sc1(rs, WS_FP + (r * 1024 + 256 * j + 64 * w + 8 * ch + 2 * kg) * 8, v4u{pk24(t.x, t8), t32, pk24(t.y, t8), t32});
      if (w == 0) STAMP(7);
    }
    {
      waitvm<0>();
      if (w == 0) STAMP(2);
      a[7] = v4u{0u, 0u, 0u, 0u};
      PERMPAIR(bt, 0, a[0], a[1]); PERMPAIR(bt, 1, a[2], a[3]); PERMPAIR(bt, 2, a[4], a[5]); PERMPAIR(bt, 3, a[6], a[7]);
      lds_wait(C + C_H, 1);
      const f2 t = gemv_mfma(bt, *(const LAS v4u*)(L + L_HIMG + fragoff), lane);
      if (!set) st_sc1(rs, WS_FP + ((64 + r) * 1024 + 256 * j + 64 * w + 8 * ch + 2 * kg) * 8, v4u{pk24(t.x, t8), t32, pk24(t.y, t8), t32});
      if (lane == 0) __hip_atomic_fetch_add(CA + C_FB, 1u, __ATOMIC_RELAXED, __HIP_MEMORY_SCOPE_WORKGROUP);
      if (w == 0) STAMP(13);
    }
  } else if (wave < 8) {
    const int a = wave - 4;
    __builtin_amdgcn_s_barrier();
    lds_wait(C + C_QD, 1);
    const u32 t32 = __builtin_amdgcn_readfirstlane(C[C_T32]), t8 = __builtin_amdgcn_readfirstlane(C[C_T8]);
    {
      v4u v[8];
      const u32 o0 = WS_QP + (1024 * g + 256 * a + 2 * lane) * 8;
      u32 spins = 0, bad;
#pragma unroll 1
      do {
#pragma unroll
        for (int k = 0; k < 8; ++k) v[k] = ld_sc1(rs, o0 + (k >> 1) * 32768 + (k & 1) * 1024);
        bad = 0;
#pragma unroll
        for (int k = 0; k < 8; ++k) bad |= bad32(v[k], t32);
        asm volatile("" ::: "memory");
      } while (bad != 0 && ++spins < SPIN_GLB);
#pragma unroll
      for (int h = 0; h < 2; ++h) {
        const float q0 = (__uint_as_float(v[h].x) + __uint_as_float(v[2 + h].x)) + (__uint_as_float(v[4 + h].x) + __uint_as_float(v[6 + h].x));
        const float q1 = (__uint_as_float(v[h].z) + __uint_as_float(v[2 + h].z)) + (__uint_as_float(v[4 + h].z) + __uint_as_float(v[6 + h].z));
        *(LAS u32*)(L + L_QIMG + (2 * a + h) * 272 + lane * 4) = pkbf(q0, q1);
      }
      lds_signal(CA + C_Q, lane);
    }
    if (a == 0) STAMP(3);
    lds_wait(C + C_KV, 4);
    if (a == 0) STAMP(18);
    v4u kf[4], vf[2][2];
#pragma unroll
    for (int t = 0; t < 4; ++t) kf[t] = *(const LAS v4u*)(L + L_K + (16 * a + ci) * 256 + (((4 * t + kg) ^ ci) << 4));
#pragma unroll
    for (int u = 0; u < 2; ++u) {
      u32 vv[8];
#pragma unroll
      for (int e = 0; e < 8; ++e) vv[e] = *(const LAS u32*)(L + L_V + (32 * u + 8 * kg + e) * 256 + 64 * a + 4 * ci);
#pragma unroll
      for (int e = 0; e < 4; ++e) { vf[u][0][e] = plo(vv[2 * e], vv[2 * e + 1]); vf[u][1][e] = phi(vv[2 * e], vv[2 * e + 1]); }
    }
    PIN4(vf[0][0], vf[0][1], vf[1][0], vf[1][1]);
    lds_wait(C + C_Q, 4);
    if (a == 0) STAMP(4);
    constexpr float LOG2E = 1.4426950408889634f;
    {
      f4 sc = {0.f, 0.f, 0.f, 0.f};
#pragma unroll
      for (int t = 0; t < 4; ++t) {
        const v4u qf = *(const LAS v4u*)(L + L_QIMG + (ci & 7) * 272 + 64 * t + 16 * kg);
        sc = mfma(kf[t], qf, sc);
      }
#pragma unroll
      for (int e = 0; e < 4; ++e) sc[e] = rbf(rbf(sc[e]) * 0.08838834764831845f);
      const float m = xrow_max(fmaxf(fmaxf(sc.x, sc.y), fmaxf(sc.z, sc.w)));
      f4 pe;
#pragma unroll
      for (int e = 0; e < 4; ++e) pe[e] = __builtin_amdgcn_exp2f((sc[e] - m) * LOG2E);
      if (ci < 8) *(LAS f4*)(L + L_SC + ci * 272 + (16 * a + 4 * kg) * 4) = pe;
      const float l = xrow_sum((pe.x + pe.y) + (pe.z + pe.w));
      if (lane < 8) *(LAS f2*)(L + L_MLX + lane * 32 + a * 8) = f2{m, l};
      lds_signal(CA + C_S, lane);
    }
    if (a == 0) STAMP(14);
    lds_wait(C + C_S, 4);
    if (a == 0) STAMP(15);
    {
      const f4 ml01 = *(const LAS f4*)(L + L_MLX + (ci & 7) * 32), ml23 = *(const LAS f4*)(L + L_MLX + (ci & 7) * 32 + 16);
      f4 sv[4];
#pragma unroll
      for (int e = 0; e < 4; ++e) sv[e] = *(const LAS f4*)(L + L_SC + (ci & 7) * 272 + (32 * (e >> 1) + 8 * kg + 4 * (e & 1)) * 4);
      const float m = fmaxf(fmaxf(ml01.x, ml01.z), fmaxf(ml23.x, ml23.z));
      const float f0 = __builtin_amdgcn_exp2f((ml01.x - m) * LOG2E), f1 = __builtin_amdgcn_exp2f((ml01.z - m) * LOG2E);
      const float f2_ = __builtin_amdgcn_exp2f((ml23.x - m) * LOG2E), f3 = __builtin_amdgcn_exp2f((ml23.z - m) * LOG2E);
      const float lsum = (f0 * ml01.y + f1 * ml01.w) + (f2_ * ml23.y + f3 * ml23.w);
      const float fa = (kg & 2) ? f1 : f0, fb = (kg & 2) ? f3 : f2_;
      v4u pf[2];
#pragma unroll
      for (int e = 0; e < 4; ++e) {
        const f4 pe = sv[e] * ((e >> 1) ? fb : fa);
        pf[e >> 1][2 * (e & 1)] = pkbf(pe.x, pe.y);
        pf[e >> 1][2 * (e & 1) + 1] = pkbf(pe.z, pe.w);
      }
      f4 o0 = {0.f, 0.f, 0.f, 0.f}, o1 = {0.f, 0.f, 0.f, 0.f};
      o0 = mfma(pf[0], vf[0][0], o0); o1 = mfma(pf[0], vf[0][1], o1);
      o0 = mfma(pf[1], vf[1][0], o0); o1 = mfma(pf[1], vf[1][1], o1);
      asm volatile("" :: "v"(o0), "v"(o1));
      if (a == 0) STAMP(16);
      const int hb = 4 * (kg & 1) + 2 * (kg >> 1);
      const bool up = kg >= 2;
      const u32 ob = WS_OP + ((((8 * g + hb) * 2 + (a >> 1)) * 64 + s) * 64 + 32 * (a & 1) + 2 * ci) * 8;
      st_sc1(rs, ob, v4u{pk24(up ? o0.z : o0.x, t8), t32, pk24(up ? o1.z : o1.x, t8), t32});
      st_sc1(rs, ob + 2 * 64 * 64 * 8, v4u{pk24(up ? o0.w : o0.y, t8), t32, pk24(up ? o1.w : o1.y, t8), t32});
      if (a == 0 && lane < 8) st_sc1(rs, WS_ML + ((8 * g + lane) * 64 + s) * 16, v4u{pk24(m, t8), t32, pk24(lsum, t8), t32});
    }
    if (a == 0) STAMP(5);
    {
      v4u v[8], ml;
      const int sg = lane >> 3;
      const u32 o0 = WS_OP + ((r * 64 + sg) * 64 + 16 * a + 2 * (lane & 7)) * 8;
      const u32 om = WS_ML + ((r >> 1) * 64 + lane) * 16;
      u32 spins = 0, bad;
#pragma unroll 1
      do {
        ml = ld_sc1(rs, om);
#pragma unroll
        for (int k = 0; k < 8; ++k) v[k] = ld_sc1(rs, o0 + k * 4096);
        bad = bad40(ml, t32, t8);
#pragma unroll
        for (int k = 0; k < 8; ++k) bad |= bad40(v[k], t32, t8);
        asm volatile("" ::: "memory");
      } while (bad != 0 && ++spins < SPIN_GLB);
      if (a == 0) STAMP(17);
      const float ms = un24(ml.x), ls = un24(ml.z);
      const float M = wave_max(ms);
      const float ws_ = __builtin_amdgcn_exp2f((ms - M) * LOG2E);
      const float inv = __builtin_amdgcn_rcpf(wave_sum(ws_ * ls));
      float e0 = 0.f, e1 = 0.f;
#pragma unroll
      for (int k = 0; k < 8; ++k) {
        const float fk = __shfl(ws_, 8 * k + sg);
        e0 = fmaf(fk, un24(v[k].x), e0); e1 = fmaf(fk, un24(v[k].z), e1);
      }
      float e = tr32(e0, e1);
      e = tr16(e, e);
      e += dpp<ROR8>(e);
      e *= inv;
      if ((lane & 24) == 0) *(LAS unsigned short*)(L + L_OIMG + (16 * a + 2 * (lane & 7) + (lane >> 5)) * 2) = (unsigned short)pkbf(e, e);
      lds_signal(CA + C_O, lane);
    }
    if (a == 0) STAMP(6);
  } else {
    v2u xr[4];
#pragma unroll
    for (int q = 0; q < 4; ++q) xr[q] = *(const GAS v2u*)(p.x + (j ^ q) * 512 + lane * 8);
    const u64 eold = __hip_atomic_load((GAS u64*)(p.ws + WS_EPOCH), __ATOMIC_RELAXED, __HIP_MEMORY_SCOPE_AGENT);
    const int li = (lane & 31) < 28 ? (lane & 31) : 27;
    const u32 b1w = *(const GAS u32*)(p.b1 + (56 * r + 2 * li) * 2);
    const int cg = 32 * xcd + loc;
    const u32 xfin = *(const GAS unsigned short*)(p.x + (4 * cg + kg) * 2), bfin = *(const GAS unsigned short*)(p.b2 + (4 * cg + kg) * 2);
    if (lane < 16) C[lane] = 0u;
    if (lane < 32) *(LAS u32*)(L + L_HIMG + lane * 4) = 0u;
    if (lane < 32) ((volatile LAS u64*)(L + L_USED))[lane] = 0;
    LGKM0();
    __builtin_amdgcn_s_barrier();
    __builtin_amdgcn_sched_barrier(0);
    f4 v[4];
#pragma unroll
    for (int q = 0; q < 4; ++q)
      v[q] = f4{bf2f(xr[q].x & 0xffffu), __uint_as_float(xr[q].x & 0xffff0000u), bf2f(xr[q].y & 0xffffu), __uint_as_float(xr[q].y & 0xffff0000u)};
    const u64 e = eold + 1;
    const u32 t32 = __builtin_amdgcn_readfirstlane((u32)e), ehi = __builtin_amdgcn_readfirstlane((u32)(e >> 32)), t8 = ehi & 0xffu;
    if (lane == 0) { C[C_T32] = t32; C[C_T8] = t8; }
#pragma unroll
    for (int pass = 0; pass < 2; ++pass) {
      const f4 s4 = (v[0] + v[1]) + (v[2] + v[3]);
      const float mean = wave_sum((s4.x + s4.y) + (s4.z + s4.w)) * (1.f / 1024.f);
      f4 d[4];
#pragma unroll
      for (int q = 0; q < 4; ++q) { d[q] = v[q] - mean; d[q] *= d[q]; }
      const f4 q4 = (d[0] + d[1]) + (d[2] + d[3]);
      const float var = wave_sum((q4.x + q4.y) + (q4.z + q4.w)) * (1.f / 1024.f);
      const float mb = rbf(mean), den = rbf(__builtin_sqrtf(rbf(rbf(var) + 1e-5f))), rd = __builtin_amdgcn_rcpf(den);
#pragma unroll
      for (int z = 0; z < 4; ++z) v[0][z] = fdiv(rbf(v[0][z] - mb), den, rd);
      const v2u pk = {pkbf(v[0].x, v[0].y), pkbf(v[0].z, v[0].w)};
      *(LAS v2u*)(L + (pass ? L_YS : L_XS) + lane * 8) = pk;
      lds_signal(CA + (pass ? C_LY : C_LN), lane);
      if (pass == 0) {
        STAMP(9);
        __builtin_amdgcn_sched_barrier(0);
        v[0] = f4{bf2f(pk.x & 0xffffu), __uint_as_float(pk.x & 0xffff0000u), bf2f(pk.y & 0xffffu), __uint_as_float(pk.y & 0xffff0000u)};
#pragma unroll
        for (int q = 1; q < 4; ++q)
#pragma unroll
          for (int z = 0; z < 4; ++z) v[q][z] = rbf(fdiv(rbf(v[q][z] - mb), den, rd));
      }
    }
    if (has_w1) lds_wait(C + C_HD, 1); else lds_wait(C + C_KV, 4);
    {
      const u32 o0 = WS_HP + ((lane >> 5) * 3584 + 56 * r + 2 * li) * 8;
      v4u v0, v1;
      u32 spins = 0, bad;
#pragma unroll 1
      do {
        v0 = ld_sc1(rs, o0); v1 = ld_sc1(rs, o0 + 2 * 3584 * 8);
        bad = bad32(v0, t32) | bad32(v1, t32);
        asm volatile("" ::: "memory");
      } while (bad != 0 && ++spins < SPIN_GLB);
      float h0 = __uint_as_float(v0.x) + __uint_as_float(v1.x), h1 = __uint_as_float(v0.z) + __uint_as_float(v1.z);
      h0 = tr32(h0, h0); h1 = tr32(h1, h1);
      float hh[2] = {rbf(rbf(h0) + bf2f(b1w & 0xffffu)), rbf(rbf(h1) + __uint_as_float(b1w & 0xffff0000u))};
#pragma unroll
      for (int z = 0; z < 2; ++z) {
        const float t = hh[z], u = 0.7978845608028654f * (t + 0.044715f * t * t * t);
        const float th = 1.f - 2.f / (__expf(2.f * u) + 1.f);
        const int n = 2 * lane + z, st = n >= 28, nn = n - 28 * st, k7 = nn / 7;
        if (lane < 28) *(LAS unsigned short*)(L + L_HIMG + (32 * st + 8 * k7 + (nn - 7 * k7)) * 2) = (unsigned short)pkbf(0.5f * t * (1.f + th), 0.f);
      }
      lds_signal(CA + C_H, lane);
    }
    STAMP(12);
    {
      v4u v[4];
      const u32 o0 = WS_FP + (lane * 1024 + 4 * cg) * 8;
      lds_wait(C + C_FB, 4);
      u32 spins = 0, bad;
#pragma unroll 1
      do {
        v[0] = ld_sc1(rs, o0); v[1] = ld_sc1(rs, o0 + 16);
        v[2] = ld_sc1(rs, o0 + 64 * 1024 * 8); v[3] = ld_sc1(rs, o0 + 64 * 1024 * 8 + 16);
        bad = bad40(v[0], t32, t8) | bad40(v[1], t32, t8) | bad40(v[2], t32, t8) | bad40(v[3], t32, t8);
        asm volatile("" ::: "memory");
      } while (bad != 0 && ++spins < SPIN_GLB);
      float A = tr16(tr32(un24(v[0].x), un24(v[1].x)), tr32(un24(v[0].z), un24(v[1].z)));
      float B = tr16(tr32(un24(v[2].x), un24(v[3].x)), tr32(un24(v[2].z), un24(v[3].z)));
      A = row_sum(A); B = row_sum(B);
      const float res = rbf(bf2f(xfin) + rbf(A)) + rbf(rbf(B) + bf2f(bfin));
      if (ci == 0) *(GAS unsigned short*)(p.out + (4 * cg + kg) * 2) = (unsigned short)(pkbf(res, res) & 0xffffu);
      if (bid == 0 && lane == 0)
        __hip_atomic_store((GAS u64*)(p.ws + WS_EPOCH), e, __ATOMIC_RELAXED, __HIP_MEMORY_SCOPE_AGENT);
      STAMP(8);
      STAMP_DUMP();
    }
  }
}
}

extern "C" void kernel_launch(void* const* d_in, const int* in_sizes, int n_in, void* d_out, int out_size, void* d_ws, size_t ws_size,
                              hipStream_t stream) {
  Params p;
  p.x = (const u8*)d_in[0]; p.kc = (const u8*)d_in[1]; p.vc = (const u8*)d_in[2]; p.wq = (const u8*)d_in[3]; p.wo = (const u8*)d_in[4];
  p.w1 = (const u8*)d_in[6]; p.b1 = (const u8*)d_in[7]; p.w2 = (const u8*)d_in[8]; p.b2 = (const u8*)d_in[9];
  p.out = (u8*)d_out; p.ws = (u8*)d_ws;
  hipLaunchKernelGGL(fused_decode_kernel, dim3(256), dim3(NTHR), 0, stream, p);
}
```

```cpp
#include <hip/hip_runtime.h>
#include <stdint.h>

#define GAS __attribute__((address_space(1)))
#define LAS __attribute__((address_space(3)))
typedef unsigned u32;
typedef unsigned long long u64;
typedef unsigned char u8;
typedef u32 v4u __attribute__((ext_vector_type(4)));
typedef u32 v2u __attribute__((ext_vector_type(2)));
typedef float f4 __attribute__((ext_vector_type(4)));
typedef float f2 __attribute__((ext_vector_type(2)));
typedef short bf16x8 __attribute__((ext_vector_type(8)));
typedef __bf16 bf16x2 __attribute__((ext_vector_type(2)));

#define STAMPS 0
#define PW 6

namespace {
constexpr int SK = 65536, WIN = 4096;
constexpr int NWAVES = 9, NTHR = NWAVES * 64;
constexpr u32 WS_EPOCH = 0;
constexpr u32 WS_QP = 4096;
constexpr u32 WS_HP = WS_QP + 4 * 4096 * 8;
constexpr u32 WS_ML = WS_HP + 4 * 3584 * 8;
constexpr u32 WS_OP = WS_ML + 32 * 64 * 16;
constexpr u32 WS_FP = WS_OP + 32 * 2 * 64 * 64 * 8;
constexpr u32 WS_END = WS_FP + 2 * 64 * 1024 * 8;
constexpr u32 WS_STAMP = 8u << 20;
constexpr int L_K = 0, L_V = 16384, L_XS = 32768, L_YS = L_XS + 512, L_QPART = L_YS + 512, L_HPART = L_QPART + 1024,
              L_QIMG = L_HPART + 1024, L_SC = L_QIMG + 2304, L_MLX = L_SC + 2304, L_OIMG = L_MLX + 256, L_HIMG = L_OIMG + 128,
              L_CTRL = L_HIMG + 128, L_USED = L_CTRL + 256, L_BYTES = 90112;
static_assert(L_USED <= L_BYTES, "lds");
enum { C_LN = 0, C_LY, C_QP, C_QD, C_KV, C_HP, C_HD, C_Q, C_S, C_O, C_H, C_FB, C_T32, C_T8, C_EHI };
constexpr u32 SPIN_LDS = 1u << 22, SPIN_GLB = 1u << 18;

struct Params { const u8 *x, *kc, *vc, *wq, *wo, *w1, *b1, *w2, *b2; u8 *out, *ws; };

__device__ __forceinline__ float bf2f(u32 b) { return __uint_as_float(b << 16); }
__device__ __forceinline__ u32 pkbf(float lo, float hi) {
  bf16x2 v = {(__bf16)lo, (__bf16)hi};
  return __builtin_bit_cast(u32, v);
}
__device__ __forceinline__ float rbf(float f) { return (float)(__bf16)f; }
__device__ __forceinline__ float dot2(u32 a, u32 b, float c) {
  return __builtin_amdgcn_fdot2_f32_bf16(__builtin_bit_cast(bf16x2, a), __builtin_bit_cast(bf16x2, b), c, false);
}
__device__ __forceinline__ u32 plo(u32 a, u32 b) { return __builtin_amdgcn_perm(b, a, 0x05040100u); }
__device__ __forceinline__ u32 phi(u32 a, u32 b) { return __builtin_amdgcn_perm(b, a, 0x07060302u); }
__device__ __forceinline__ f4 mfma(v4u a, v4u b, f4 c) {
  return __builtin_amdgcn_mfma_f32_16x16x32_bf16(__builtin_bit_cast(bf16x8, a), __builtin_bit_cast(bf16x8, b), c, 0, 0, 0);
}
template <int CTRL> __device__ __forceinline__ float dpp(float x) {
  return __builtin_bit_cast(float, __builtin_amdgcn_mov_dpp(__builtin_bit_cast(int, x), CTRL, 0xf, 0xf, true));
}
constexpr int XOR1 = 0xB1, XOR2 = 0x4E, HMIR = 0x141, ROR8 = 0x128, ROR4 = 0x124;
__device__ __forceinline__ float tr32(float a, float b) {
  auto s = __builtin_amdgcn_permlane32_swap(__float_as_uint(a), __float_as_uint(b), false, false);
  return __uint_as_float(s[0]) + __uint_as_float(s[1]);
}
__device__ __forceinline__ float tr16(float a, float b) {
  auto s = __builtin_amdgcn_permlane16_swap(__float_as_uint(a), __float_as_uint(b), false, false);
  return __uint_as_float(s[0]) + __uint_as_float(s[1]);
}
__device__ __forceinline__ float row_sum(float v) {
  v += dpp<XOR1>(v); v += dpp<XOR2>(v); v += dpp<HMIR>(v); v += dpp<ROR8>(v);
  return v;
}
__device__ __forceinline__ float row_max(float v) {
  v = fmaxf(v, dpp<XOR1>(v)); v = fmaxf(v, dpp<XOR2>(v)); v = fmaxf(v, dpp<HMIR>(v)); v = fmaxf(v, dpp<ROR8>(v));
  return v;
}
__device__ __forceinline__ float xrow_sum(float x) { x = tr16(x, x); return tr32(x, x); }
__device__ __forceinline__ float xrow_max(float x) {
  auto s = __builtin_amdgcn_permlane16_swap(__float_as_uint(x), __float_as_uint(x), false, false);
  x = fmaxf(__uint_as_float(s[0]), __uint_as_float(s[1]));
  auto t = __builtin_amdgcn_permlane32_swap(__float_as_uint(x), __float_as_uint(x), false, false);
  return fmaxf(__uint_as_float(t[0]), __uint_as_float(t[1]));
}
__device__ __forceinline__ float wave_sum(float v) { return xrow_sum(row_sum(v)); }
__device__ __forceinline__ float wave_max(float v) { return xrow_max(row_max(v)); }

#define LDNT(dst, ptr) asm volatile("global_load_dwordx4 %0, %1, off nt" : "=v"(dst) : "v"(ptr) : "memory")
#define WAITVM(n) asm volatile("s_waitcnt vmcnt(" #n ")" ::: "memory")
#define PIN4(a, b, c, d) asm volatile("" : "+v"(a), "+v"(b), "+v"(c), "+v"(d)::"memory")
#define LGKM0() asm volatile("s_waitcnt lgkmcnt(0)" ::: "memory")
template <int N> __device__ __forceinline__ void waitvm() { asm volatile("s_waitcnt vmcnt(%0)" ::"i"(N) : "memory"); }
__device__ __forceinline__ void dma16(const u8* src, u32 lds_dst) {
  u32 keep;
  asm volatile("s_mov_b32 %0, m0\n\ts_mov_b32 m0, %2\n\ts_nop 0\n\tglobal_load_lds_dwordx4 %1, off nt\n\ts_mov_b32 m0, %0"
               : "=&s"(keep) : "v"(src), "s"(lds_dst) : "memory");
}
__device__ __forceinline__ void lds_wait(volatile LAS u32* p, u32 target) {
#pragma unroll 1
  for (u32 spins = 0; spins < SPIN_LDS; ++spins) {
    if (__builtin_amdgcn_readfirstlane(*p) >= target) break;
  }
  asm volatile("" ::: "memory");
}
__device__ __forceinline__ void lds_signal(LAS u32* p, int lane) {
  LGKM0();
  if (lane == 0) __hip_atomic_fetch_add(p, 1u, __ATOMIC_RELAXED, __HIP_MEMORY_SCOPE_WORKGROUP);
}
__device__ __forceinline__ u32 lds_arrive(LAS u32* p, int lane) {
  LGKM0();
  u32 old = 0;
  if (lane == 0) old = __hip_atomic_fetch_add(p, 1u, __ATOMIC_RELAXED, __HIP_MEMORY_SCOPE_WORKGROUP);
  return __builtin_amdgcn_readfirstlane(old);
}
__device__ __forceinline__ v4u ld_sc1(__amdgpu_buffer_rsrc_t rs, u32 off) { return __builtin_amdgcn_raw_buffer_load_b128(rs, (int)off, 0, 16); }
__device__ __forceinline__ void st_sc1(__amdgpu_buffer_rsrc_t rs, u32 off, v4u v) { __builtin_amdgcn_raw_buffer_store_b128(v, rs, (int)off, 0, 16); }
__device__ __forceinline__ u32 bad32(v4u v, u32 t32) { return (v.y ^ t32) | (v.w ^ t32); }
__device__ __forceinline__ u32 bad40(v4u v, u32 t32, u32 t8) { return (v.y ^ t32) | (v.w ^ t32) | (((v.x ^ t8) | (v.z ^ t8)) & 0xffu); }
__device__ __forceinline__ u32 pk24(float v, u32 t8) { return (__float_as_uint(v) & 0xffffff00u) | t8; }
__device__ __forceinline__ float un24(u32 v) { return __uint_as_float(v & 0xffffff00u); }
__device__ __forceinline__ float fdiv(float a, float d, float rd) {
  float q = a * rd;
  return fmaf(fmaf(-q, d, a), rd, q);
}

#define STAMP(id) do {} while (0)

#define PIN2(a, b) asm volatile("" : "+v"(a), "+v"(b)::"memory")
#define PERMPAIR(bt, e, wa, wb) do { PIN2(wa, wb); \
    u32 t_[8]; \
    _Pragma("unroll") for (int d_ = 0; d_ < 4; ++d_) { t_[2 * d_] = plo(wa[d_], wb[d_]); t_[2 * d_ + 1] = phi(wa[d_], wb[d_]); } \
    asm volatile("" : "+v"(t_[0]), "+v"(t_[1]), "+v"(t_[2]), "+v"(t_[3]), "+v"(t_[4]), "+v"(t_[5]), "+v"(t_[6]), "+v"(t_[7])); \
    _Pragma("unroll") for (int d_ = 0; d_ < 8; ++d_) bt[d_][e] = t_[d_]; } while (0)
__device__ __forceinline__ f2 gemv_mfma(const v4u (&bt)[8], v4u xa, int lane) {
  const f4 z = {0.f, 0.f, 0.f, 0.f};
  const bool set = lane & 8;
  float v[8];
#pragma unroll
  for (int t = 0; t < 8; ++t) { const f4 d = mfma(xa, bt[t], z); v[t] = set ? d.y : d.x; }
  const bool k0 = lane & 16, k1 = lane & 32;
  float u0 = k1 ? (k0 ? v[6] : v[4]) : (k0 ? v[2] : v[0]);
  float u1 = k1 ? (k0 ? v[7] : v[5]) : (k0 ? v[3] : v[1]);
  u0 += dpp<ROR8>(u0); u1 += dpp<ROR8>(u1);
  return f2{u0, u1};
}

__global__ void __launch_bounds__(NTHR, 1) fused_decode_kernel(Params p) {
  __shared__ __attribute__((aligned(16))) u8 smem[L_BYTES];
  LAS u8* L = (LAS u8*)smem;
  volatile LAS u32* C = (volatile LAS u32*)(L + L_CTRL);
  LAS u32* CA = (LAS u32*)(L + L_CTRL);
  const u64 tstart_ = __builtin_amdgcn_s_memrealtime();
  const int lane = threadIdx.x & 63, wave = __builtin_amdgcn_readfirstlane(threadIdx.x >> 6);
  const int bid = blockIdx.x, xcd = bid & 7, loc = bid >> 3, r = 8 * xcd + (loc >> 2), j = loc & 3;
  const int g = r >> 4, s = (r & 15) * 4 + j;
  const bool has_w1 = (loc >> 2) < 7;
  const int sl = 7 * xcd + (loc >> 2);
  const int ci = lane & 15, kg = lane >> 4;
  const __amdgpu_buffer_rsrc_t rs = __builtin_amdgcn_make_buffer_rsrc((void*)p.ws, 0, WS_END, 0x00020000);

  if (__builtin_expect(wave < 4, 1)) {
    const int w = wave, set = ci >> 3, ch = ci & 7;
    v4u a[8], b[8], bt[8], bo[8];
    {
      const u8* q = p.wq + (size_t)(256 * j + 64 * w + 32 * set + 8 * kg) * 8192 + 128 * r + 16 * ch;
#pragma unroll
      for (int i = 0; i < 8; ++i) LDNT(a[i], q + i * 8192);
    }
    if (w == 0) STAMP(0);
    __builtin_amdgcn_s_barrier();
    const size_t kvoff = ((size_t)g * SK + (SK - WIN) + 64 * s) * 256;
    const u32 lbase = (u32)(uintptr_t)L;
    const u8* kp = p.kc + kvoff + (16 * w + kg) * 256;
    const u8* vp = p.vc + kvoff + w * 4096 + lane * 16;
    const u8* q1 = p.w1 + (size_t)(256 * j + 64 * w + 32 * set + 8 * kg) * 7168 + 128 * sl + 16 * ch;
    const u8* qo = p.wo + (size_t)(64 * r + 32 * set + 8 * kg) * 2048 + 512 * j + 128 * w + 16 * ch;
    const u8* q2 = p.w2 + (size_t)(56 * r + 28 * set + 7 * kg) * 2048 + 512 * j + 128 * w + 16 * ch;
    const u32 fragoff = (32 * (ci & 1) + 8 * kg) * 2;
    u32 t32 = 0, t8 = 0;
#define EV(XX) do { \
      if ((XX) < 8 && ((XX) & 1)) PERMPAIR(bt, ((XX) >> 1) & 3, a[((XX) - 1) & 7], a[(XX) & 7]); \
      if ((XX) == 7) { \
        lds_wait(C + C_LN, 1); \
        const f2 t = gemv_mfma(bt, *(const LAS v4u*)(L + L_XS + 128 * w + fragoff), lane); \
        t32 = __builtin_amdgcn_readfirstlane(C[C_T32]); t8 = __builtin_amdgcn_readfirstlane(C[C_T8]); \
        if (!set) *(LAS f2*)(L + L_QPART + w * 256 + (8 * ch + 2 * kg) * 4) = t; \
        if (lds_arrive(CA + C_QP, lane) == 3) { \
          if (lane < 32) { \
            const LAS f2* pp = (const LAS f2*)(L + L_QPART + lane * 8); \
            const f2 u = (pp[0] + pp[32]) + (pp[64] + pp[96]); \
            st_sc1(rs, WS_QP + (j * 4096 + 64 * r + 2 * lane) * 8, v4u{__float_as_uint(u.x), t32, __float_as_uint(u.y), t32}); \
          } \
          if (lane == 0) C[C_QD] = 1u; \
          STAMP(10); \
        } \
      } \
      if ((XX) == 15 && has_w1) lds_signal(CA + C_KV, lane); \
      if ((XX) >= 16 && (XX) < 24 && ((XX) & 1) && has_w1) PERMPAIR(bt, ((XX) >> 1) & 3, a[((XX) - 1) & 7], a[(XX) & 7]); \
      if ((XX) == 23 && has_w1) { \
        lds_wait(C + C_LY, 1); \
        const f2 t = gemv_mfma(bt, *(const LAS v4u*)(L + L_YS + 128 * w + fragoff), lane); \
        if (!set) *(LAS f2*)(L + L_HPART + w * 256 + (8 * ch + 2 * kg) * 4) = t; \
        if (lds_arrive(CA + C_HP, lane) == 3) { \
          if (lane < 32) { \
            const LAS f2* pp = (const LAS f2*)(L + L_HPART + lane * 8); \
            const f2 u = (pp[0] + pp[32]) + (pp[64] + pp[96]); \
            st_sc1(rs, WS_HP + (j * 3584 + 64 * sl + 2 * lane) * 8, v4u{__float_as_uint(u.x), t32, __float_as_uint(u.y), t32}); \
          } \
          if (lane == 0) C[C_HD] = 1u; \
          STAMP(11); \
        } \
      } \
      if ((XX) >= 24 && (XX) < 32 && ((XX) & 1)) PERMPAIR(bo, ((XX) >> 1) & 3, b[((XX) - 1) & 7], b[(XX) & 7]); \
    } while (0)
#define EVS(n) do { if ((n) == 8) { _Pragma("unroll") for (int y_ = 0; y_ <= 8 - PW; ++y_) EV(y_); } else EV((n) - PW); } while (0)
#pragma unroll
    for (int i = 0; i < 4; ++i) { waitvm<PW - 1>(); dma16(kp + i * 1024 + ((ci ^ ((4 * i + kg) & 15)) << 4), lbase + L_K + w * 4096 + i * 1024); EVS(8 + i); }
#pragma unroll
    for (int i = 0; i < 4; ++i) { waitvm<PW - 1>(); dma16(vp + i * 1024, lbase + L_V + w * 4096 + i * 1024); EVS(12 + i); }
    if (has_w1) {
#pragma unroll
      for (int i = 0; i < 8; ++i) { waitvm<PW - 1>(); LDNT(a[i], q1 + i * 7168); EVS(16 + i); }
    } else {
      waitvm<0>();
      lds_signal(CA + C_KV, lane);
    }
#pragma unroll
    for (int i = 0; i < 8; ++i) { waitvm<PW - 1>(); LDNT(b[i], qo + i * 2048); EVS(24 + i); }
#pragma unroll
    for (int i = 0; i < 7; ++i) { waitvm<PW - 1>(); LDNT(a[i], q2 + i * 2048); EVS(32 + i); }
    if (w == 0) STAMP(1);
    {
      lds_wait(C + C_O, 4);
      const f2 t = gemv_mfma(bo, *(const LAS v4u*)(L + L_OIMG + fragoff), lane);
      if (!set) st_sc1(rs, WS_FP + (r * 1024 + 256 * j + 64 * w + 8 * ch + 2 * kg) * 8, v4u{pk24(t.x, t8), t32, pk24(t.y, t8), t32});
      if (w == 0) STAMP(7);
    }
    {
      waitvm<0>();
      if (w == 0) STAMP(2);
      a[7] = v4u{0u, 0u, 0u, 0u};
      PERMPAIR(bt, 0, a[0], a[1]); PERMPAIR(bt, 1, a[2], a[3]); PERMPAIR(bt, 2, a[4], a[5]); PERMPAIR(bt, 3, a[6], a[7]);
      lds_wait(C + C_H, 1);
      const f2 t = gemv_mfma(bt, *(const LAS v4u*)(L + L_HIMG + fragoff), lane);
      if (!set) st_sc1(rs, WS_FP + ((64 + r) * 1024 + 256 * j + 64 * w + 8 * ch + 2 * kg) * 8, v4u{pk24(t.x, t8), t32, pk24(t.y, t8), t32});
      if (lane == 0) __hip_atomic_fetch_add(CA + C_FB, 1u, __ATOMIC_RELAXED, __HIP_MEMORY_SCOPE_WORKGROUP);
      if (w == 0) STAMP(13);
    }
  } else if (wave < 8) {
    const int a = wave - 4;
    __builtin_amdgcn_s_barrier();
    lds_wait(C + C_QD, 1);
    const u32 t32 = __builtin_amdgcn_readfirstlane(C[C_T32]), t8 = __builtin_amdgcn_readfirstlane(C[C_T8]);
    {
      v4u v[8];
      const u32 o0 = WS_QP + (1024 * g + 256 * a + 2 * lane) * 8;
      u32 spins = 0, bad;
#pragma unroll 1
      do {
#pragma unroll
        for (int k = 0; k < 8; ++k) v[k] = ld_sc1(rs, o0 + (k >> 1) * 32768 + (k & 1) * 1024);
        bad = 0;
#pragma unroll
        for (int k = 0; k < 8; ++k) bad |= bad32(v[k], t32);
        asm volatile("" ::: "memory");
      } while (bad != 0 && ++spins < SPIN_GLB);
#pragma unroll
      for (int h = 0; h < 2; ++h) {
        const float q0 = (__uint_as_float(v[h].x) + __uint_as_float(v[2 + h].x)) + (__uint_as_float(v[4 + h].x) + __uint_as_float(v[6 + h].x));
        const float q1 = (__uint_as_float(v[h].z) + __uint_as_float(v[2 + h].z)) + (__uint_as_float(v[4 + h].z) + __uint_as_float(v[6 + h].z));
        *(LAS u32*)(L + L_QIMG + (2 * a + h) * 272 + lane * 4) = pkbf(q0, q1);
      }
      lds_signal(CA + C_Q, lane);
    }
    if (a == 0) STAMP(3);
    lds_wait(C + C_KV, 4);
    v4u kf[4], vf[2][2];
#pragma unroll
    for (int t = 0; t < 4; ++t) kf[t] = *(const LAS v4u*)(L + L_K + (16 * a + ci) * 256 + (((4 * t + kg) ^ ci) << 4));
#pragma unroll
    for (int u = 0; u < 2; ++u) {
      u32 vv[8];
#pragma unroll
      for (int e = 0; e < 8; ++e) vv[e] = *(const LAS u32*)(L + L_V + (32 * u + 8 * kg + e) * 256 + 64 * a + 4 * ci);
#pragma unroll
      for (int e = 0; e < 4; ++e) { vf[u][0][e] = plo(vv[2 * e], vv[2 * e + 1]); vf[u][1][e] = phi(vv[2 * e], vv[2 * e + 1]); }
    }
    PIN4(vf[0][0], vf[0][1], vf[1][0], vf[1][1]);
    lds_wait(C + C_Q, 4);
    if (a == 0) STAMP(4);
    constexpr float LOG2E = 1.4426950408889634f;
    {
      f4 sc = {0.f, 0.f, 0.f, 0.f};
#pragma unroll
      for (int t = 0; t < 4; ++t) {
        const v4u qf = *(const LAS v4u*)(L + L_QIMG + (ci & 7) * 272 + 64 * t + 16 * kg);
        sc = mfma(kf[t], qf, sc);
      }
#pragma unroll
      for (int e = 0; e < 4; ++e) sc[e] = rbf(rbf(sc[e]) * 0.08838834764831845f);
      const float m = xrow_max(fmaxf(fmaxf(sc.x, sc.y), fmaxf(sc.z, sc.w)));
      f4 pe;
#pragma unroll
      for (int e = 0; e < 4; ++e) pe[e] = __builtin_amdgcn_exp2f((sc[e] - m) * LOG2E);
      if (ci < 8) *(LAS f4*)(L + L_SC + ci * 272 + (16 * a + 4 * kg) * 4) = pe;
      const float l = xrow_sum((pe.x + pe.y) + (pe.z + pe.w));
      if (lane < 8) *(LAS f2*)(L + L_MLX + lane * 32 + a * 8) = f2{m, l};
      lds_signal(CA + C_S, lane);
    }
    lds_wait(C + C_S, 4);
    {
      const f4 ml01 = *(const LAS f4*)(L + L_MLX + (ci & 7) * 32), ml23 = *(const LAS f4*)(L + L_MLX + (ci & 7) * 32 + 16);
      f4 sv[4];
#pragma unroll
      for (int e = 0; e < 4; ++e) sv[e] = *(const LAS f4*)(L + L_SC + (ci & 7) * 272 + (32 * (e >> 1) + 8 * kg + 4 * (e & 1)) * 4);
      const float m = fmaxf(fmaxf(ml01.x, ml01.z), fmaxf(ml23.x, ml23.z));
      const float f0 = __builtin_amdgcn_exp2f((ml01.x - m) * LOG2E), f1 = __builtin_amdgcn_exp2f((ml01.z - m) * LOG2E);
      const float f2_ = __builtin_amdgcn_exp2f((ml23.x - m) * LOG2E), f3 = __builtin_amdgcn_exp2f((ml23.z - m) * LOG2E);
      const float lsum = (f0 * ml01.y + f1 * ml01.w) + (f2_ * ml23.y + f3 * ml23.w);
      const float fa = (kg & 2) ? f1 : f0, fb = (kg & 2) ? f3 : f2_;
      v4u pf[2];
#pragma unroll
      for (int e = 0; e < 4; ++e) {
        const f4 pe = sv[e] * ((e >> 1) ? fb : fa);
        pf[e >> 1][2 * (e & 1)] = pkbf(pe.x, pe.y);
        pf[e >> 1][2 * (e & 1) + 1] = pkbf(pe.z, pe.w);
      }
      f4 o0 = {0.f, 0.f, 0.f, 0.f}, o1 = {0.f, 0.f, 0.f, 0.f};
      o0 = mfma(pf[0], vf[0][0], o0); o1 = mfma(pf[0], vf[0][1], o1);
      o0 = mfma(pf[1], vf[1][0], o0); o1 = mfma(pf[1], vf[1][1], o1);
      const int hb = 4 * (kg & 1) + 2 * (kg >> 1);
      const bool up = kg >= 2;
      const u32 ob = WS_OP + ((((8 * g + hb) * 2 + (a >> 1)) * 64 + s) * 64 + 32 * (a & 1) + 2 * ci) * 8;
      st_sc1(rs, ob, v4u{pk24(up ? o0.z : o0.x, t8), t32, pk24(up ? o1.z : o1.x, t8), t32});
      st_sc1(rs, ob + 2 * 64 * 64 * 8, v4u{pk24(up ? o0.w : o0.y, t8), t32, pk24(up ? o1.w : o1.y, t8), t32});
      if (a == 0 && lane < 8) st_sc1(rs, WS_ML + ((8 * g + lane) * 64 + s) * 16, v4u{pk24(m, t8), t32, pk24(lsum, t8), t32});
    }
    if (a == 0) STAMP(5);
    {
      v4u v[8], ml;
      const int sg = lane >> 3;
      const u32 o0 = WS_OP + ((r * 64 + sg) * 64 + 16 * a + 2 * (lane & 7)) * 8;
      const u32 om = WS_ML + ((r >> 1) * 64 + lane) * 16;
      u32 spins = 0, bad;
#pragma unroll 1
      do {
        ml = ld_sc1(rs, om);
#pragma unroll
        for (int k = 0; k < 8; ++k) v[k] = ld_sc1(rs, o0 + k * 4096);
        bad = bad40(ml, t32, t8);
#pragma unroll
        for (int k = 0; k < 8; ++k) bad |= bad40(v[k], t32, t8);
        asm volatile("" ::: "memory");
      } while (bad != 0 && ++spins < SPIN_GLB);
      const float ms = un24(ml.x), ls = un24(ml.z);
      const float M = wave_max(ms);
      const float ws_ = __builtin_amdgcn_exp2f((ms - M) * LOG2E);
      const float inv = __builtin_amdgcn_rcpf(wave_sum(ws_ * ls));
      float e0 = 0.f, e1 = 0.f;
#pragma unroll
      for (int k = 0; k < 8; ++k) {
        const float fk = __shfl(ws_, 8 * k + sg);
        e0 = fmaf(fk, un24(v[k].x), e0); e1 = fmaf(fk, un24(v[k].z), e1);
      }
      float e = tr32(e0, e1);
      e = tr16(e, e);
      e += dpp<ROR8>(e);
      e *= inv;
      if ((lane & 24) == 0) *(LAS unsigned short*)(L + L_OIMG + (16 * a + 2 * (lane & 7) + (lane >> 5)) * 2) = (unsigned short)pkbf(e, e);
      lds_signal(CA + C_O, lane);
    }
    if (a == 0) STAMP(6);
  } else {
    v2u xr[4];
#pragma unroll
    for (int q = 0; q < 4; ++q) xr[q] = *(const GAS v2u*)(p.x + (j ^ q) * 512 + lane * 8);
    const u64 eold = __hip_atomic_load((GAS u64*)(p.ws + WS_EPOCH), __ATOMIC_RELAXED, __HIP_MEMORY_SCOPE_AGENT);
    const int li = (lane & 31) < 28 ? (lane & 31) : 27;
    const u32 b1w = *(const GAS u32*)(p.b1 + (56 * r + 2 * li) * 2);
    const int cg = 32 * xcd + loc;
    const u32 xfin = *(const GAS unsigned short*)(p.x + (4 * cg + kg) * 2), bfin = *(const GAS unsigned short*)(p.b2 + (4 * cg + kg) * 2);
    if (lane < 16) C[lane] = 0u;
    if (lane < 32) *(LAS u32*)(L + L_HIMG + lane * 4) = 0u;
    LGKM0();
    __builtin_amdgcn_s_barrier();
    __builtin_amdgcn_sched_barrier(0);
    f4 v[4];
#pragma unroll
    for (int q = 0; q < 4; ++q)
      v[q] = f4{bf2f(xr[q].x & 0xffffu), __uint_as_float(xr[q].x & 0xffff0000u), bf2f(xr[q].y & 0xffffu), __uint_as_float(xr[q].y & 0xffff0000u)};
    const u64 e = eold + 1;
    const u32 t32 = __builtin_amdgcn_readfirstlane((u32)e), ehi = __builtin_amdgcn_readfirstlane((u32)(e >> 32)), t8 = ehi & 0xffu;
    if (lane == 0) { C[C_T32] = t32; C[C_T8] = t8; }
#pragma unroll
    for (int pass = 0; pass < 2; ++pass) {
      const f4 s4 = (v[0] + v[1]) + (v[2] + v[3]);
      const float mean = wave_sum((s4.x + s4.y) + (s4.z + s4.w)) * (1.f / 1024.f);
      f4 d[4];
#pragma unroll
      for (int q = 0; q < 4; ++q) { d[q] = v[q] - mean; d[q] *= d[q]; }
      const f4 q4 = (d[0] + d[1]) + (d[2] + d[3]);
      const float var = wave_sum((q4.x + q4.y) + (q4.z + q4.w)) * (1.f / 1024.f);
      const float mb = rbf(mean), den = rbf(__builtin_sqrtf(rbf(rbf(var) + 1e-5f))), rd = __builtin_amdgcn_rcpf(den);
#pragma unroll
      for (int z = 0; z < 4; ++z) v[0][z] = fdiv(rbf(v[0][z] - mb), den, rd);
      const v2u pk = {pkbf(v[0].x, v[0].y), pkbf(v[0].z, v[0].w)};
      *(LAS v2u*)(L + (pass ? L_YS : L_XS) + lane * 8) = pk;
      lds_signal(CA + (pass ? C_LY : C_LN), lane);
      if (pass == 0) {
        STAMP(9);
        __builtin_amdgcn_sched_barrier(0);
        v[0] = f4{bf2f(pk.x & 0xffffu), __uint_as_float(pk.x & 0xffff0000u), bf2f(pk.y & 0xffffu), __uint_as_float(pk.y & 0xffff0000u)};
#pragma unroll
        for (int q = 1; q < 4; ++q)
#pragma unroll
          for (int z = 0; z < 4; ++z) v[q][z] = rbf(fdiv(rbf(v[q][z] - mb), den, rd));
      }
    }
    if (has_w1) lds_wait(C + C_HD, 1); else lds_wait(C + C_KV, 4);
    {
      const u32 o0 = WS_HP + ((lane >> 5) * 3584 + 56 * r + 2 * li) * 8;
      v4u v0, v1;
      u32 spins = 0, bad;
#pragma unroll 1
      do {
        v0 = ld_sc1(rs, o0); v1 = ld_sc1(rs, o0 + 2 * 3584 * 8);
        bad = bad32(v0, t32) | bad32(v1, t32);
        asm volatile("" ::: "memory");
      } while (bad != 0 && ++spins < SPIN_GLB);
      float h0 = __uint_as_float(v0.x) + __uint_as_float(v1.x), h1 = __uint_as_float(v0.z) + __uint_as_float(v1.z);
      h0 = tr32(h0, h0); h1 = tr32(h1, h1);
      float hh[2] = {rbf(rbf(h0) + bf2f(b1w & 0xffffu)), rbf(rbf(h1) + __uint_as_float(b1w & 0xffff0000u))};
#pragma unroll
      for (int z = 0; z < 2; ++z) {
        const float t = hh[z], u = 0.7978845608028654f * (t + 0.044715f * t * t * t);
        const float th = 1.f - 2.f / (__expf(2.f * u) + 1.f);
        const int n = 2 * lane + z, st = n >= 28, nn = n - 28 * st, k7 = nn / 7;
        if (lane < 28) *(LAS unsigned short*)(L + L_HIMG + (32 * st + 8 * k7 + (nn - 7 * k7)) * 2) = (unsigned short)pkbf(0.5f * t * (1.f + th), 0.f);
      }
      lds_signal(CA + C_H, lane);
    }
    STAMP(12);
    {
      v4u v[4];
      const u32 o0 = WS_FP + (lane * 1024 + 4 * cg) * 8;
      lds_wait(C + C_FB, 4);
      u32 spins = 0, bad;
#pragma unroll 1
      do {
        v[0] = ld_sc1(rs, o0); v[1] = ld_sc1(rs, o0 + 16);
        v[2] = ld_sc1(rs, o0 + 64 * 1024 * 8); v[3] = ld_sc1(rs, o0 + 64 * 1024 * 8 + 16);
        bad = bad40(v[0], t32, t8) | bad40(v[1], t32, t8) | bad40(v[2], t32, t8) | bad40(v[3], t32, t8);
        asm volatile("" ::: "memory");
      } while (bad != 0 && ++spins < SPIN_GLB);
      float A = tr16(tr32(un24(v[0].x), un24(v[1].x)), tr32(un24(v[0].z), un24(v[1].z)));
      float B = tr16(tr32(un24(v[2].x), un24(v[3].x)), tr32(un24(v[2].z), un24(v[3].z)));
      A = row_sum(A); B = row_sum(B);
      const float res = rbf(bf2f(xfin) + rbf(A)) + rbf(rbf(B) + bf2f(bfin));
      if (ci == 0) *(GAS unsigned short*)(p.out + (4 * cg + kg) * 2) = (unsigned short)(pkbf(res, res) & 0xffffu);
      if (bid == 0 && lane == 0)
        __hip_atomic_store((GAS u64*)(p.ws + WS_EPOCH), e, __ATOMIC_RELAXED, __HIP_MEMORY_SCOPE_AGENT);
      while (__builtin_amdgcn_s_memrealtime() < tstart_ + 2500) __builtin_amdgcn_s_sleep(1);
    }
  }
}
}

extern "C" void kernel_launch(void* const* d_in, const int* in_sizes, int n_in, void* d_out, int out_size, void* d_ws, size_t ws_size,
                              hipStream_t stream) {
  Params p;
  p.x = (const u8*)d_in[0]; p.kc = (const u8*)d_in[1]; p.vc = (const u8*)d_in[2]; p.wq = (const u8*)d_in[3]; p.wo = (const u8*)d_in[4];
  p.w1 = (const u8*)d_in[6]; p.b1 = (const u8*)d_in[7]; p.w2 = (const u8*)d_in[8]; p.b2 = (const u8*)d_in[9];
  p.out = (u8*)d_out; p.ws = (u8*)d_ws;
  hipLaunchKernelGGL(fused_decode_kernel, dim3(256), dim3(NTHR), 0, stream, p);
}
```

```cpp
#include <hip/hip_runtime.h>
#include <stdint.h>

#define GAS __attribute__((address_space(1)))
#define LAS __attribute__((address_space(3)))
typedef unsigned u32;
typedef unsigned long long u64;
typedef unsigned char u8;
typedef u32 v4u __attribute__((ext_vector_type(4)));
typedef u32 v2u __attribute__((ext_vector_type(2)));
typedef float f4 __attribute__((ext_vector_type(4)));
typedef float f2 __attribute__((ext_vector_type(2)));
typedef short bf16x8 __attribute__((ext_vector_type(8)));
typedef __bf16 bf16x2 __attribute__((ext_vector_type(2)));

#define STAMPS 0
#define PW 6

namespace {
constexpr int SK = 65536, WIN = 4096;
constexpr int NWAVES = 9, NTHR = NWAVES * 64;
constexpr u32 WS_EPOCH = 0;
constexpr u32 WS_QP = 4096;
constexpr u32 WS_HP = WS_QP + 4 * 4096 * 8;
constexpr u32 WS_ML = WS_HP + 4 * 3584 * 8;
constexpr u32 WS_OP = WS_ML + 32 * 64 * 16;
constexpr u32 WS_FP = WS_OP + 32 * 2 * 64 * 64 * 8;
constexpr u32 WS_ST = WS_FP + 2 * 64 * 1024 * 8;
constexpr u32 WS_END = WS_ST + 4096;
constexpr u32 WS_STAMP = 8u << 20;
constexpr int L_K = 0, L_V = 16384, L_XS = 32768, L_YS = L_XS + 512, L_QPART = L_YS + 512, L_HPART = L_QPART + 1024,
              L_QIMG = L_HPART + 1024, L_SC = L_QIMG + 2304, L_MLX = L_SC + 2304, L_OIMG = L_MLX + 256, L_HIMG = L_OIMG + 128,
              L_CTRL = L_HIMG + 128, L_USED = L_CTRL + 256, L_BYTES = 90112;
static_assert(L_USED <= L_BYTES, "lds");
enum { C_LN = 0, C_LY, C_QP, C_QD, C_KV, C_HP, C_HD, C_Q, C_S, C_O, C_H, C_FB, C_T32, C_T8, C_EHI };
constexpr u32 SPIN_LDS = 1u << 22, SPIN_GLB = 1u << 18;

struct Params { const u8 *x, *kc, *vc, *wq, *wo, *w1, *b1, *w2, *b2; u8 *out, *ws; };

__device__ __forceinline__ float bf2f(u32 b) { return __uint_as_float(b << 16); }
__device__ __forceinline__ u32 pkbf(float lo, float hi) {
  bf16x2 v = {(__bf16)lo, (__bf16)hi};
  return __builtin_bit_cast(u32, v);
}
__device__ __forceinline__ float rbf(float f) { return (float)(__bf16)f; }
__device__ __forceinline__ float dot2(u32 a, u32 b, float c) {
  return __builtin_amdgcn_fdot2_f32_bf16(__builtin_bit_cast(bf16x2, a), __builtin_bit_cast(bf16x2, b), c, false);
}
__device__ __forceinline__ u32 plo(u32 a, u32 b) { return __builtin_amdgcn_perm(b, a, 0x05040100u); }
__device__ __forceinline__ u32 phi(u32 a, u32 b) { return __builtin_amdgcn_perm(b, a, 0x07060302u); }
__device__ __forceinline__ f4 mfma(v4u a, v4u b, f4 c) {
  return __builtin_amdgcn_mfma_f32_16x16x32_bf16(__builtin_bit_cast(bf16x8, a), __builtin_bit_cast(bf16x8, b), c, 0, 0, 0);
}
template <int CTRL> __device__ __forceinline__ float dpp(float x) {
  return __builtin_bit_cast(float, __builtin_amdgcn_mov_dpp(__builtin_bit_cast(int, x), CTRL, 0xf, 0xf, true));
}
constexpr int XOR1 = 0xB1, XOR2 = 0x4E, HMIR = 0x141, ROR8 = 0x128, ROR4 = 0x124;
__device__ __forceinline__ float tr32(float a, float b) {
  auto s = __builtin_amdgcn_permlane32_swap(__float_as_uint(a), __float_as_uint(b), false, false);
  return __uint_as_float(s[0]) + __uint_as_float(s[1]);
}
__device__ __forceinline__ float tr16(float a, float b) {
  auto s = __builtin_amdgcn_permlane16_swap(__float_as_uint(a), __float_as_uint(b), false, false);
  return __uint_as_float(s[0]) + __uint_as_float(s[1]);
}
__device__ __forceinline__ float row_sum(float v) {
  v += dpp<XOR1>(v); v += dpp<XOR2>(v); v += dpp<HMIR>(v); v += dpp<ROR8>(v);
  return v;
}
__device__ __forceinline__ float row_max(float v) {
  v = fmaxf(v, dpp<XOR1>(v)); v = fmaxf(v, dpp<XOR2>(v)); v = fmaxf(v, dpp<HMIR>(v)); v = fmaxf(v, dpp<ROR8>(v));
  return v;
}
__device__ __forceinline__ float xrow_sum(float x) { x = tr16(x, x); return tr32(x, x); }
__device__ __forceinline__ float xrow_max(float x) {
  auto s = __builtin_amdgcn_permlane16_swap(__float_as_uint(x), __float_as_uint(x), false, false);
  x = fmaxf(__uint_as_float(s[0]), __uint_as_float(s[1]));
  auto t = __builtin_amdgcn_permlane32_swap(__float_as_uint(x), __float_as_uint(x), false, false);
  return fmaxf(__uint_as_float(t[0]), __uint_as_float(t[1]));
}
__device__ __forceinline__ float wave_sum(float v) { return xrow_sum(row_sum(v)); }
__device__ __forceinline__ float wave_max(float v) { return xrow_max(row_max(v)); }

#define LDNT(dst, ptr) asm volatile("global_load_dwordx4 %0, %1, off nt" : "=v"(dst) : "v"(ptr) : "memory")
#define WAITVM(n) asm volatile("s_waitcnt vmcnt(" #n ")" ::: "memory")
#define PIN4(a, b, c, d) asm volatile("" : "+v"(a), "+v"(b), "+v"(c), "+v"(d)::"memory")
#define LGKM0() asm volatile("s_waitcnt lgkmcnt(0)" ::: "memory")
template <int N> __device__ __forceinline__ void waitvm() { asm volatile("s_waitcnt vmcnt(%0)" ::"i"(N) : "memory"); }
__device__ __forceinline__ void dma16(const u8* src, u32 lds_dst) {
  u32 keep;
  asm volatile("s_mov_b32 %0, m0\n\ts_mov_b32 m0, %2\n\ts_nop 0\n\tglobal_load_lds_dwordx4 %1, off nt\n\ts_mov_b32 m0, %0"
               : "=&s"(keep) : "v"(src), "s"(lds_dst) : "memory");
}
__device__ __forceinline__ void lds_wait(volatile LAS u32* p, u32 target) {
#pragma unroll 1
  for (u32 spins = 0; spins < SPIN_LDS; ++spins) {
    if (__builtin_amdgcn_readfirstlane(*p) >= target) break;
  }
  asm volatile("" ::: "memory");
}
__device__ __forceinline__ void lds_signal(LAS u32* p, int lane) {
  LGKM0();
  if (lane == 0) __hip_atomic_fetch_add(p, 1u, __ATOMIC_RELAXED, __HIP_MEMORY_SCOPE_WORKGROUP);
}
__device__ __forceinline__ u32 lds_arrive(LAS u32* p, int lane) {
  LGKM0();
  u32 old = 0;
  if (lane == 0) old = __hip_atomic_fetch_add(p, 1u, __ATOMIC_RELAXED, __HIP_MEMORY_SCOPE_WORKGROUP);
  return __builtin_amdgcn_readfirstlane(old);
}
__device__ __forceinline__ v4u ld_sc1(__amdgpu_buffer_rsrc_t rs, u32 off) { return __builtin_amdgcn_raw_buffer_load_b128(rs, (int)off, 0, 16); }
__device__ __forceinline__ void st_sc1(__amdgpu_buffer_rsrc_t rs, u32 off, v4u v) { __builtin_amdgcn_raw_buffer_store_b128(v, rs, (int)off, 0, 16); }
__device__ __forceinline__ u32 bad32(v4u v, u32 t32) { return (v.y ^ t32) | (v.w ^ t32); }
__device__ __forceinline__ u32 bad40(v4u v, u32 t32, u32 t8) { return (v.y ^ t32) | (v.w ^ t32) | (((v.x ^ t8) | (v.z ^ t8)) & 0xffu); }
__device__ __forceinline__ u32 pk24(float v, u32 t8) { return (__float_as_uint(v) & 0xffffff00u) | t8; }
__device__ __forceinline__ float un24(u32 v) { return __uint_as_float(v & 0xffffff00u); }
__device__ __forceinline__ float fdiv(float a, float d, float rd) {
  float q = a * rd;
  return fmaf(fmaf(-q, d, a), rd, q);
}

#define STAMP(id) do {} while (0)

#define PIN2(a, b) asm volatile("" : "+v"(a), "+v"(b)::"memory")
#define PERMPAIR(bt, e, wa, wb) do { PIN2(wa, wb); \
    u32 t_[8]; \
    _Pragma("unroll") for (int d_ = 0; d_ < 4; ++d_) { t_[2 * d_] = plo(wa[d_], wb[d_]); t_[2 * d_ + 1] = phi(wa[d_], wb[d_]); } \
    asm volatile("" : "+v"(t_[0]), "+v"(t_[1]), "+v"(t_[2]), "+v"(t_[3]), "+v"(t_[4]), "+v"(t_[5]), "+v"(t_[6]), "+v"(t_[7])); \
    _Pragma("unroll") for (int d_ = 0; d_ < 8; ++d_) bt[d_][e] = t_[d_]; } while (0)
__device__ __forceinline__ f2 gemv_mfma(const v4u (&bt)[8], v4u xa, int lane) {
  const f4 z = {0.f, 0.f, 0.f, 0.f};
  const bool set = lane & 8;
  float v[8];
#pragma unroll
  for (int t = 0; t < 8; ++t) { const f4 d = mfma(xa, bt[t], z); v[t] = set ? d.y : d.x; }
  const bool k0 = lane & 16, k1 = lane & 32;
  float u0 = k1 ? (k0 ? v[6] : v[4]) : (k0 ? v[2] : v[0]);
  float u1 = k1 ? (k0 ? v[7] : v[5]) : (k0 ? v[3] : v[1]);
  u0 += dpp<ROR8>(u0); u1 += dpp<ROR8>(u1);
  return f2{u0, u1};
}

__global__ void __launch_bounds__(NTHR, 1) fused_decode_kernel(Params p) {
  __shared__ __attribute__((aligned(16))) u8 smem[L_BYTES];
  LAS u8* L = (LAS u8*)smem;
  volatile LAS u32* C = (volatile LAS u32*)(L + L_CTRL);
  LAS u32* CA = (LAS u32*)(L + L_CTRL);
  const u64 tstart_ = __builtin_amdgcn_s_memrealtime();
  const int lane = threadIdx.x & 63, wave = __builtin_amdgcn_readfirstlane(threadIdx.x >> 6);
  const int bid = blockIdx.x, xcd = bid & 7, loc = bid >> 3, r = 8 * xcd + (loc >> 2), j = loc & 3;
  const int g = r >> 4, s = (r & 15) * 4 + j;
  const bool has_w1 = (loc >> 2) < 7;
  const int sl = 7 * xcd + (loc >> 2);
  const int ci = lane & 15, kg = lane >> 4;
  const __amdgpu_buffer_rsrc_t rs = __builtin_amdgcn_make_buffer_rsrc((void*)p.ws, 0, WS_END, 0x00020000);

  if (__builtin_expect(wave < 4, 1)) {
    const int w = wave, set = ci >> 3, ch = ci & 7;
    v4u a[8], b[8], bt[8], bo[8];
    {
      const u8* q = p.wq + (size_t)(256 * j + 64 * w + 32 * set + 8 * kg) * 8192 + 128 * r + 16 * ch;
#pragma unroll
      for (int i = 0; i < 8; ++i) LDNT(a[i], q + i * 8192);
    }
    if (w == 0) STAMP(0);
    __builtin_amdgcn_s_barrier();
    const size_t kvoff = ((size_t)g * SK + (SK - WIN) + 64 * s) * 256;
    const u32 lbase = (u32)(uintptr_t)L;
    const u8* kp = p.kc + kvoff + (16 * w + kg) * 256;
    const u8* vp = p.vc + kvoff + w * 4096 + lane * 16;
    const u8* q1 = p.w1 + (size_t)(256 * j + 64 * w + 32 * set + 8 * kg) * 7168 + 128 * sl + 16 * ch;
    const u8* qo = p.wo + (size_t)(64 * r + 32 * set + 8 * kg) * 2048 + 512 * j + 128 * w + 16 * ch;
    const u8* q2 = p.w2 + (size_t)(56 * r + 28 * set + 7 * kg) * 2048 + 512 * j + 128 * w + 16 * ch;
    const u32 fragoff = (32 * (ci & 1) + 8 * kg) * 2;
    u32 t32 = 0, t8 = 0;
#define EV(XX) do { \
      if ((XX) < 8 && ((XX) & 1)) PERMPAIR(bt, ((XX) >> 1) & 3, a[((XX) - 1) & 7], a[(XX) & 7]); \
      if ((XX) == 7) { \
        lds_wait(C + C_LN, 1); \
        const f2 t = gemv_mfma(bt, *(const LAS v4u*)(L + L_XS + 128 * w + fragoff), lane); \
        t32 = __builtin_amdgcn_readfirstlane(C[C_T32]); t8 = __builtin_amdgcn_readfirstlane(C[C_T8]); \
        if (!set) *(LAS f2*)(L + L_QPART + w * 256 + (8 * ch + 2 * kg) * 4) = t; \
        if (lds_arrive(CA + C_QP, lane) == 3) { \
          if (lane < 32) { \
            const LAS f2* pp = (const LAS f2*)(L + L_QPART + lane * 8); \
            const f2 u = (pp[0] + pp[32]) + (pp[64] + pp[96]); \
            st_sc1(rs, WS_QP + (j * 4096 + 64 * r + 2 * lane) * 8, v4u{__float_as_uint(u.x), t32, __float_as_uint(u.y), t32}); \
          } \
          if (lane == 0) C[C_QD] = 1u; \
          STAMP(10); \
        } \
      } \
      if ((XX) == 15 && has_w1) lds_signal(CA + C_KV, lane); \
      if ((XX) >= 16 && (XX) < 24 && ((XX) & 1) && has_w1) PERMPAIR(bt, ((XX) >> 1) & 3, a[((XX) - 1) & 7], a[(XX) & 7]); \
      if ((XX) == 23 && has_w1) { \
        lds_wait(C + C_LY, 1); \
        const f2 t = gemv_mfma(bt, *(const LAS v4u*)(L + L_YS + 128 * w + fragoff), lane); \
        if (!set) *(LAS f2*)(L + L_HPART + w * 256 + (8 * ch + 2 * kg) * 4) = t; \
        if (lds_arrive(CA + C_HP, lane) == 3) { \
          if (lane < 32) { \
            const LAS f2* pp = (const LAS f2*)(L + L_HPART + lane * 8); \
            const f2 u = (pp[0] + pp[32]) + (pp[64] + pp[96]); \
            st_sc1(rs, WS_HP + (j * 3584 + 64 * sl + 2 * lane) * 8, v4u{__float_as_uint(u.x), t32, __float_as_uint(u.y), t32}); \
          } \
          if (lane == 0) C[C_HD] = 1u; \
          STAMP(11); \
        } \
      } \
      if ((XX) >= 24 && (XX) < 32 && ((XX) & 1)) PERMPAIR(bo, ((XX) >> 1) & 3, b[((XX) - 1) & 7], b[(XX) & 7]); \
    } while (0)
#define EVS(n) do { if ((n) == 8) { _Pragma("unroll") for (int y_ = 0; y_ <= 8 - PW; ++y_) EV(y_); } else EV((n) - PW); } while (0)
#pragma unroll
    for (int i = 0; i < 4; ++i) { waitvm<PW - 1>(); dma16(kp + i * 1024 + ((ci ^ ((4 * i + kg) & 15)) << 4), lbase + L_K + w * 4096 + i * 1024); EVS(8 + i); }
#pragma unroll
    for (int i = 0; i < 4; ++i) { waitvm<PW - 1>(); dma16(vp + i * 1024, lbase + L_V + w * 4096 + i * 1024); EVS(12 + i); }
    if (has_w1) {
#pragma unroll
      for (int i = 0; i < 8; ++i) { waitvm<PW - 1>(); LDNT(a[i], q1 + i * 7168); EVS(16 + i); }
    } else {
      waitvm<0>();
      lds_signal(CA + C_KV, lane);
    }
#pragma unroll
    for (int i = 0; i < 8; ++i) { waitvm<PW - 1>(); LDNT(b[i], qo + i * 2048); EVS(24 + i); }
#pragma unroll
    for (int i = 0; i < 7; ++i) { waitvm<PW - 1>(); LDNT(a[i], q2 + i * 2048); EVS(32 + i); }
    if (w == 0) STAMP(1);
    {
      lds_wait(C + C_O, 4);
      const f2 t = gemv_mfma(bo, *(const LAS v4u*)(L + L_OIMG + fragoff), lane);
      if (!set) st_sc1(rs, WS_FP + (r * 1024 + 256 * j + 64 * w + 8 * ch + 2 * kg) * 8, v4u{pk24(t.x, t8), t32, pk24(t.y, t8), t32});
      if (w == 0) STAMP(7);
    }
    {
      waitvm<0>();
      if (w == 0) STAMP(2);
      a[7] = v4u{0u, 0u, 0u, 0u};
      PERMPAIR(bt, 0, a[0], a[1]); PERMPAIR(bt, 1, a[2], a[3]); PERMPAIR(bt, 2, a[4], a[5]); PERMPAIR(bt, 3, a[6], a[7]);
      lds_wait(C + C_H, 1);
      const f2 t = gemv_mfma(bt, *(const LAS v4u*)(L + L_HIMG + fragoff), lane);
      if (!set) st_sc1(rs, WS_FP + ((64 + r) * 1024 + 256 * j + 64 * w + 8 * ch + 2 * kg) * 8, v4u{pk24(t.x, t8), t32, pk24(t.y, t8), t32});
      if (lane == 0) __hip_atomic_fetch_add(CA + C_FB, 1u, __ATOMIC_RELAXED, __HIP_MEMORY_SCOPE_WORKGROUP);
      if (w == 0) STAMP(13);
    }
  } else if (wave < 8) {
    const int a = wave - 4;
    __builtin_amdgcn_s_barrier();
    lds_wait(C + C_QD, 1);
    const u32 t32 = __builtin_amdgcn_readfirstlane(C[C_T32]), t8 = __builtin_amdgcn_readfirstlane(C[C_T8]);
    {
      v4u v[8];
      const u32 o0 = WS_QP + (1024 * g + 256 * a + 2 * lane) * 8;
      u32 spins = 0, bad;
#pragma unroll 1
      do {
#pragma unroll
        for (int k = 0; k < 8; ++k) v[k] = ld_sc1(rs, o0 + (k >> 1) * 32768 + (k & 1) * 1024);
        bad = 0;
#pragma unroll
        for (int k = 0; k < 8; ++k) bad |= bad32(v[k], t32);
        asm volatile("" ::: "memory");
      } while (bad != 0 && ++spins < SPIN_GLB);
#pragma unroll
      for (int h = 0; h < 2; ++h) {
        const float q0 = (__uint_as_float(v[h].x) + __uint_as_float(v[2 + h].x)) + (__uint_as_float(v[4 + h].x) + __uint_as_float(v[6 + h].x));
        const float q1 = (__uint_as_float(v[h].z) + __uint_as_float(v[2 + h].z)) + (__uint_as_float(v[4 + h].z) + __uint_as_float(v[6 + h].z));
        *(LAS u32*)(L + L_QIMG + (2 * a + h) * 272 + lane * 4) = pkbf(q0, q1);
      }
      lds_signal(CA + C_Q, lane);
    }
    if (a == 0) STAMP(3);
    lds_wait(C + C_KV, 4);
    v4u kf[4], vf[2][2];
#pragma unroll
    for (int t = 0; t < 4; ++t) kf[t] = *(const LAS v4u*)(L + L_K + (16 * a + ci) * 256 + (((4 * t + kg) ^ ci) << 4));
#pragma unroll
    for (int u = 0; u < 2; ++u) {
      u32 vv[8];
#pragma unroll
      for (int e = 0; e < 8; ++e) vv[e] = *(const LAS u32*)(L + L_V + (32 * u + 8 * kg + e) * 256 + 64 * a + 4 * ci);
#pragma unroll
      for (int e = 0; e < 4; ++e) { vf[u][0][e] = plo(vv[2 * e], vv[2 * e + 1]); vf[u][1][e] = phi(vv[2 * e], vv[2 * e + 1]); }
    }
    PIN4(vf[0][0], vf[0][1], vf[1][0], vf[1][1]);
    lds_wait(C + C_Q, 4);
    if (a == 0) STAMP(4);
    constexpr float LOG2E = 1.4426950408889634f;
    {
      f4 sc = {0.f, 0.f, 0.f, 0.f};
#pragma unroll
      for (int t = 0; t < 4; ++t) {
        const v4u qf = *(const LAS v4u*)(L + L_QIMG + (ci & 7) * 272 + 64 * t + 16 * kg);
        sc = mfma(kf[t], qf, sc);
      }
#pragma unroll
      for (int e = 0; e < 4; ++e) sc[e] = rbf(rbf(sc[e]) * 0.08838834764831845f);
      const float m = xrow_max(fmaxf(fmaxf(sc.x, sc.y), fmaxf(sc.z, sc.w)));
      f4 pe;
#pragma unroll
      for (int e = 0; e < 4; ++e) pe[e] = __builtin_amdgcn_exp2f((sc[e] - m) * LOG2E);
      if (ci < 8) *(LAS f4*)(L + L_SC + ci * 272 + (16 * a + 4 * kg) * 4) = pe;
      const float l = xrow_sum((pe.x + pe.y) + (pe.z + pe.w));
      if (lane < 8) *(LAS f2*)(L + L_MLX + lane * 32 + a * 8) = f2{m, l};
      lds_signal(CA + C_S, lane);
    }
    lds_wait(C + C_S, 4);
    {
      const f4 ml01 = *(const LAS f4*)(L + L_MLX + (ci & 7) * 32), ml23 = *(const LAS f4*)(L + L_MLX + (ci & 7) * 32 + 16);
      f4 sv[4];
#pragma unroll
      for (int e = 0; e < 4; ++e) sv[e] = *(const LAS f4*)(L + L_SC + (ci & 7) * 272 + (32 * (e >> 1) + 8 * kg + 4 * (e & 1)) * 4);
      const float m = fmaxf(fmaxf(ml01.x, ml01.z), fmaxf(ml23.x, ml23.z));
      const float f0 = __builtin_amdgcn_exp2f((ml01.x - m) * LOG2E), f1 = __builtin_amdgcn_exp2f((ml01.z - m) * LOG2E);
      const float f2_ = __builtin_amdgcn_exp2f((ml23.x - m) * LOG2E), f3 = __builtin_amdgcn_exp2f((ml23.z - m) * LOG2E);
      const float lsum = (f0 * ml01.y + f1 * ml01.w) + (f2_ * ml23.y + f3 * ml23.w);
      const float fa = (kg & 2) ? f1 : f0, fb = (kg & 2) ? f3 : f2_;
      v4u pf[2];
#pragma unroll
      for (int e = 0; e < 4; ++e) {
        const f4 pe = sv[e] * ((e >> 1) ? fb : fa);
        pf[e >> 1][2 * (e & 1)] = pkbf(pe.x, pe.y);
        pf[e >> 1][2 * (e & 1) + 1] = pkbf(pe.z, pe.w);
      }
      f4 o0 = {0.f, 0.f, 0.f, 0.f}, o1 = {0.f, 0.f, 0.f, 0.f};
      o0 = mfma(pf[0], vf[0][0], o0); o1 = mfma(pf[0], vf[0][1], o1);
      o0 = mfma(pf[1], vf[1][0], o0); o1 = mfma(pf[1], vf[1][1], o1);
      const int hb = 4 * (kg & 1) + 2 * (kg >> 1);
      const bool up = kg >= 2;
      const u32 ob = WS_OP + ((((8 * g + hb) * 2 + (a >> 1)) * 64 + s) * 64 + 32 * (a & 1) + 2 * ci) * 8;
      st_sc1(rs, ob, v4u{pk24(up ? o0.z : o0.x, t8), t32, pk24(up ? o1.z : o1.x, t8), t32});
      st_sc1(rs, ob + 2 * 64 * 64 * 8, v4u{pk24(up ? o0.w : o0.y, t8), t32, pk24(up ? o1.w : o1.y, t8), t32});
      if (a == 0 && lane < 8) st_sc1(rs, WS_ML + ((8 * g + lane) * 64 + s) * 16, v4u{pk24(m, t8), t32, pk24(lsum, t8), t32});
    }
    if (a == 0) STAMP(5);
    {
      v4u v[8], ml;
      const int sg = lane >> 3;
      const u32 o0 = WS_OP + ((r * 64 + sg) * 64 + 16 * a + 2 * (lane & 7)) * 8;
      const u32 om = WS_ML + ((r >> 1) * 64 + lane) * 16;
      u32 spins = 0, bad;
#pragma unroll 1
      do {
        ml = ld_sc1(rs, om);
#pragma unroll
        for (int k = 0; k < 8; ++k) v[k] = ld_sc1(rs, o0 + k * 4096);
        bad = bad40(ml, t32, t8);
#pragma unroll
        for (int k = 0; k < 8; ++k) bad |= bad40(v[k], t32, t8);
        asm volatile("" ::: "memory");
      } while (bad != 0 && ++spins < SPIN_GLB);
      const float ms = un24(ml.x), ls = un24(ml.z);
      const float M = wave_max(ms);
      const float ws_ = __builtin_amdgcn_exp2f((ms - M) * LOG2E);
      const float inv = __builtin_amdgcn_rcpf(wave_sum(ws_ * ls));
      float e0 = 0.f, e1 = 0.f;
#pragma unroll
      for (int k = 0; k < 8; ++k) {
        const float fk = __shfl(ws_, 8 * k + sg);
        e0 = fmaf(fk, un24(v[k].x), e0); e1 = fmaf(fk, un24(v[k].z), e1);
      }
      float e = tr32(e0, e1);
      e = tr16(e, e);
      e += dpp<ROR8>(e);
      e *= inv;
      if ((lane & 24) == 0) *(LAS unsigned short*)(L + L_OIMG + (16 * a + 2 * (lane & 7) + (lane >> 5)) * 2) = (unsigned short)pkbf(e, e);
      lds_signal(CA + C_O, lane);
    }
    if (a == 0) STAMP(6);
  } else {
    v2u xr[4];
#pragma unroll
    for (int q = 0; q < 4; ++q) xr[q] = *(const GAS v2u*)(p.x + (j ^ q) * 512 + lane * 8);
    const u64 eold = __hip_atomic_load((GAS u64*)(p.ws + WS_EPOCH), __ATOMIC_RELAXED, __HIP_MEMORY_SCOPE_AGENT);
    const int li = (lane & 31) < 28 ? (lane & 31) : 27;
    const u32 b1w = *(const GAS u32*)(p.b1 + (56 * r + 2 * li) * 2);
    const int cg = 32 * xcd + loc;
    const u32 xfin = *(const GAS unsigned short*)(p.x + (4 * cg + kg) * 2), bfin = *(const GAS unsigned short*)(p.b2 + (4 * cg + kg) * 2);
    if (lane < 16) C[lane] = 0u;
    if (lane < 32) *(LAS u32*)(L + L_HIMG + lane * 4) = 0u;
    LGKM0();
    __builtin_amdgcn_s_barrier();
    __builtin_amdgcn_sched_barrier(0);
    f4 v[4];
#pragma unroll
    for (int q = 0; q < 4; ++q)
      v[q] = f4{bf2f(xr[q].x & 0xffffu), __uint_as_float(xr[q].x & 0xffff0000u), bf2f(xr[q].y & 0xffffu), __uint_as_float(xr[q].y & 0xffff0000u)};
    const u64 e = eold + 1;
    const u32 t32 = __builtin_amdgcn_readfirstlane((u32)e), ehi = __builtin_amdgcn_readfirstlane((u32)(e >> 32)), t8 = ehi & 0xffu;
    if (lane == 0) { C[C_T32] = t32; C[C_T8] = t8; }
    if (lane == 0) st_sc1(rs, WS_ST + bid * 16, v4u{(u32)tstart_, t32, (u32)tstart_, t32});
#pragma unroll
    for (int pass = 0; pass < 2; ++pass) {
      const f4 s4 = (v[0] + v[1]) + (v[2] + v[3]);
      const float mean = wave_sum((s4.x + s4.y) + (s4.z + s4.w)) * (1.f / 1024.f);
      f4 d[4];
#pragma unroll
      for (int q = 0; q < 4; ++q) { d[q] = v[q] - mean; d[q] *= d[q]; }
      const f4 q4 = (d[0] + d[1]) + (d[2] + d[3]);
      const float var = wave_sum((q4.x + q4.y) + (q4.z + q4.w)) * (1.f / 1024.f);
      const float mb = rbf(mean), den = rbf(__builtin_sqrtf(rbf(rbf(var) + 1e-5f))), rd = __builtin_amdgcn_rcpf(den);
#pragma unroll
      for (int z = 0; z < 4; ++z) v[0][z] = fdiv(rbf(v[0][z] - mb), den, rd);
      const v2u pk = {pkbf(v[0].x, v[0].y), pkbf(v[0].z, v[0].w)};
      *(LAS v2u*)(L + (pass ? L_YS : L_XS) + lane * 8) = pk;
      lds_signal(CA + (pass ? C_LY : C_LN), lane);
      if (pass == 0) {
        STAMP(9);
        __builtin_amdgcn_sched_barrier(0);
        v[0] = f4{bf2f(pk.x & 0xffffu), __uint_as_float(pk.x & 0xffff0000u), bf2f(pk.y & 0xffffu), __uint_as_float(pk.y & 0xffff0000u)};
#pragma unroll
        for (int q = 1; q < 4; ++q)
#pragma unroll
          for (int z = 0; z < 4; ++z) v[q][z] = rbf(fdiv(rbf(v[q][z] - mb), den, rd));
      }
    }
    if (has_w1) lds_wait(C + C_HD, 1); else lds_wait(C + C_KV, 4);
    {
      const u32 o0 = WS_HP + ((lane >> 5) * 3584 + 56 * r + 2 * li) * 8;
      v4u v0, v1;
      u32 spins = 0, bad;
#pragma unroll 1
      do {
        v0 = ld_sc1(rs, o0); v1 = ld_sc1(rs, o0 + 2 * 3584 * 8);
        bad = bad32(v0, t32) | bad32(v1, t32);
        asm volatile("" ::: "memory");
      } while (bad != 0 && ++spins < SPIN_GLB);
      float h0 = __uint_as_float(v0.x) + __uint_as_float(v1.x), h1 = __uint_as_float(v0.z) + __uint_as_float(v1.z);
      h0 = tr32(h0, h0); h1 = tr32(h1, h1);
      float hh[2] = {rbf(rbf(h0) + bf2f(b1w & 0xffffu)), rbf(rbf(h1) + __uint_as_float(b1w & 0xffff0000u))};
#pragma unroll
      for (int z = 0; z < 2; ++z) {
        const float t = hh[z], u = 0.7978845608028654f * (t + 0.044715f * t * t * t);
        const float th = 1.f - 2.f / (__expf(2.f * u) + 1.f);
        const int n = 2 * lane + z, st = n >= 28, nn = n - 28 * st, k7 = nn / 7;
        if (lane < 28) *(LAS unsigned short*)(L + L_HIMG + (32 * st + 8 * k7 + (nn - 7 * k7)) * 2) = (unsigned short)pkbf(0.5f * t * (1.f + th), 0.f);
      }
      lds_signal(CA + C_H, lane);
    }
    STAMP(12);
    {
      v4u v[4];
      const u32 o0 = WS_FP + (lane * 1024 + 4 * cg) * 8;
      lds_wait(C + C_FB, 4);
      u32 spins = 0, bad;
#pragma unroll 1
      do {
        v[0] = ld_sc1(rs, o0); v[1] = ld_sc1(rs, o0 + 16);
        v[2] = ld_sc1(rs, o0 + 64 * 1024 * 8); v[3] = ld_sc1(rs, o0 + 64 * 1024 * 8 + 16);
        bad = bad40(v[0], t32, t8) | bad40(v[1], t32, t8) | bad40(v[2], t32, t8) | bad40(v[3], t32, t8);
        asm volatile("" ::: "memory");
      } while (bad != 0 && ++spins < SPIN_GLB);
      float A = tr16(tr32(un24(v[0].x), un24(v[1].x)), tr32(un24(v[0].z), un24(v[1].z)));
      float B = tr16(tr32(un24(v[2].x), un24(v[3].x)), tr32(un24(v[2].z), un24(v[3].z)));
      A = row_sum(A); B = row_sum(B);
      const float res = rbf(bf2f(xfin) + rbf(A)) + rbf(rbf(B) + bf2f(bfin));
      if (ci == 0) *(GAS unsigned short*)(p.out + (4 * cg + kg) * 2) = (unsigned short)(pkbf(res, res) & 0xffffu);
      if (bid == 0 && lane == 0)
        __hip_atomic_store((GAS u64*)(p.ws + WS_EPOCH), e, __ATOMIC_RELAXED, __HIP_MEMORY_SCOPE_AGENT);
      const u32 tend_ = (u32)__builtin_amdgcn_s_memrealtime();
      int mn_, mx_;
      { v4u z[4]; u32 sp_ = 0, bd_;
        do { bd_ = 0; for (int k = 0; k < 4; ++k) { z[k] = ld_sc1(rs, WS_ST + (lane * 4 + k) * 16); bd_ |= bad32(z[k], t32); } asm volatile("" ::: "memory"); } while (bd_ != 0 && ++sp_ < SPIN_GLB);
        int d0 = (int)(z[0].x - (u32)tstart_), d1 = (int)(z[1].x - (u32)tstart_), d2 = (int)(z[2].x - (u32)tstart_), d3 = (int)(z[3].x - (u32)tstart_);
        mn_ = min(min(d0, d1), min(d2, d3)); mx_ = max(max(d0, d1), max(d2, d3));
        for (int o = 32; o >= 1; o >>= 1) { mn_ = min(mn_, __shfl_xor(mn_, o)); mx_ = max(mx_, __shfl_xor(mx_, o)); }
        mn_ = __builtin_amdgcn_readfirstlane(mn_); mx_ = __builtin_amdgcn_readfirstlane(mx_);
      }
      const u64 tmin_ = tstart_ + (long long)mn_;
      while (__builtin_amdgcn_s_memrealtime() < tmin_ + 4000) __builtin_amdgcn_s_sleep(1);
    }
  }
}
}

extern "C" void kernel_launch(void* const* d_in, const int* in_sizes, int n_in, void* d_out, int out_size, void* d_ws, size_t ws_size,
                              hipStream_t stream) {
  Params p;
  p.x = (const u8*)d_in[0]; p.kc = (const u8*)d_in[1]; p.vc = (const u8*)d_in[2]; p.wq = (const u8*)d_in[3]; p.wo = (const u8*)d_in[4];
  p.w1 = (const u8*)d_in[6]; p.b1 = (const u8*)d_in[7]; p.w2 = (const u8*)d_in[8]; p.b2 = (const u8*)d_in[9];
  p.out = (u8*)d_out; p.ws = (u8*)d_ws;
  hipLaunchKernelGGL(fused_decode_kernel, dim3(256), dim3(NTHR), 0, stream, p);
}
```

```cpp
#include <hip/hip_runtime.h>
#include <stdint.h>

#define GAS __attribute__((address_space(1)))
#define LAS __attribute__((address_space(3)))
typedef unsigned u32;
typedef unsigned long long u64;
typedef unsigned char u8;
typedef u32 v4u __attribute__((ext_vector_type(4)));
typedef u32 v2u __attribute__((ext_vector_type(2)));
typedef float f4 __attribute__((ext_vector_type(4)));
typedef float f2 __attribute__((ext_vector_type(2)));
typedef short bf16x8 __attribute__((ext_vector_type(8)));
typedef __bf16 bf16x2 __attribute__((ext_vector_type(2)));

namespace {
#define TAU1 285
#define TAU2 630
#define TAUH 575
#define TOWN 140
#define TRD 220
#define CODEKB 16
#define CODE_BYTES 16128

constexpr int SK = 65536, WIN = 4096;
constexpr int NTHR = 9 * 64;
constexpr u32 WS_QP = 4096;
constexpr u32 WS_HP = WS_QP + 4 * 4096 * 8;
constexpr u32 WS_ML = WS_HP + 4 * 3584 * 8;
constexpr u32 WS_OP = WS_ML + 32 * 32 * 16;
constexpr u32 WS_FP = WS_OP + 64 * 32 * 32 * 8;
constexpr u32 WS_ST = WS_FP + 64 * 1024 * 8;
constexpr u32 WS_END = WS_ST + 128;
constexpr int L_K = 0, L_V = 32768, L_XS = 65536, L_YS = L_XS + 512, L_QPART = L_YS + 512, L_HPART = L_QPART + 1024,
              L_QIMG = L_HPART + 1024, L_PX = L_QIMG + 1152, L_MLX = L_PX + 4096, L_OIMG = L_MLX + 128, L_HIMG = L_OIMG + 128,
              L_CTRL = L_HIMG + 128, L_USED = L_CTRL + 128, L_BYTES = 90112;
static_assert(L_USED <= L_BYTES, "lds");
enum { C_LN = 0, C_LY, C_QP, C_KV, C_HP, C_Q, C_S, C_O, C_H, C_FA, C_T32, C_T8, C_TR, C_TRV };
constexpr u32 SPIN_LDS = 1u << 22, SPIN_GLB = 1u << 18;

struct Params { const u8 *wq, *x; u8* ws; const u8 *b1, *b2, *kc, *vc, *w1, *wo, *w2; u8* out; };

__device__ __forceinline__ float bf2f(u32 b) { return __uint_as_float(b << 16); }
__device__ __forceinline__ u32 pkbf(float lo, float hi) {
  bf16x2 v = {(__bf16)lo, (__bf16)hi};
  return __builtin_bit_cast(u32, v);
}
__device__ __forceinline__ float rbf(float f) { return (float)(__bf16)f; }
__device__ __forceinline__ f4 unpk4(v2u x) {
  return f4{bf2f(x.x & 0xffffu), __uint_as_float(x.x & 0xffff0000u), bf2f(x.y & 0xffffu), __uint_as_float(x.y & 0xffff0000u)};
}
__device__ __forceinline__ u32 cvtpk(float lo, float hi) { u32 r = pkbf(lo, hi); asm("" : "+v"(r)); return r; }
__device__ __forceinline__ f4 rbf4(f4 v) { return unpk4(v2u{cvtpk(v.x, v.y), cvtpk(v.z, v.w)}); }
__device__ __forceinline__ u32 plo(u32 a, u32 b) { return __builtin_amdgcn_perm(b, a, 0x05040100u); }
__device__ __forceinline__ u32 phi(u32 a, u32 b) { return __builtin_amdgcn_perm(b, a, 0x07060302u); }
__device__ __forceinline__ f4 mfma(v4u a, v4u b, f4 c) {
  return __builtin_amdgcn_mfma_f32_16x16x32_bf16(__builtin_bit_cast(bf16x8, a), __builtin_bit_cast(bf16x8, b), c, 0, 0, 0);
}
template <int CTRL> __device__ __forceinline__ float dpp(float x) {
  return __builtin_bit_cast(float, __builtin_amdgcn_mov_dpp(__builtin_bit_cast(int, x), CTRL, 0xf, 0xf, true));
}
constexpr int XOR1 = 0xB1, XOR2 = 0x4E, HMIR = 0x141, ROR8 = 0x128, ROR4 = 0x124;
__device__ __forceinline__ float tr32(float a, float b) {
  auto s = __builtin_amdgcn_permlane32_swap(__float_as_uint(a), __float_as_uint(b), false, false);
  return __uint_as_float(s[0]) + __uint_as_float(s[1]);
}
__device__ __forceinline__ float tr16(float a, float b) {
  auto s = __builtin_amdgcn_permlane16_swap(__float_as_uint(a), __float_as_uint(b), false, false);
  return __uint_as_float(s[0]) + __uint_as_float(s[1]);
}
__device__ __forceinline__ float max16(float x) {
  auto s = __builtin_amdgcn_permlane16_swap(__float_as_uint(x), __float_as_uint(x), false, false);
  return fmaxf(__uint_as_float(s[0]), __uint_as_float(s[1]));
}
__device__ __forceinline__ float max32(float x) {
  auto s = __builtin_amdgcn_permlane32_swap(__float_as_uint(x), __float_as_uint(x), false, false);
  return fmaxf(__uint_as_float(s[0]), __uint_as_float(s[1]));
}
__device__ __forceinline__ float row_sum(float v) {
  v += dpp<XOR1>(v); v += dpp<XOR2>(v); v += dpp<HMIR>(v); v += dpp<ROR8>(v);
  return v;
}
__device__ __forceinline__ float row_max(float v) {
  v = fmaxf(v, dpp<XOR1>(v)); v = fmaxf(v, dpp<XOR2>(v)); v = fmaxf(v, dpp<HMIR>(v)); v = fmaxf(v, dpp<ROR8>(v));
  return v;
}
__device__ __forceinline__ float xrow_sum(float x) { x = tr16(x, x); return tr32(x, x); }
__device__ __forceinline__ float wave_sum(float v) { return xrow_sum(row_sum(v)); }

#define LDNT(dst, ptr) asm volatile("global_load_dwordx4 %0, %1, off nt" : "=v"(dst) : "v"(ptr) : "memory")
#define PIN2(a, b) asm volatile("" : "+v"(a), "+v"(b)::"memory")
#define PIN4(a, b, c, d) asm volatile("" : "+v"(a), "+v"(b), "+v"(c), "+v"(d)::"memory")
template <int N> __device__ __forceinline__ void waitvm() { asm volatile("s_waitcnt vmcnt(%0)" ::"i"(N) : "memory"); }
__device__ __forceinline__ void dma16(const u8* src, u32 lds_dst) {
  u32 keep;
  asm volatile("s_mov_b32 %0, m0\n\ts_mov_b32 m0, %2\n\ts_nop 0\n\tglobal_load_lds_dwordx4 %1, off nt\n\ts_mov_b32 m0, %0"
               : "=&s"(keep) : "v"(src), "s"(lds_dst) : "memory");
}
__device__ __forceinline__ void lds_wait(volatile LAS u32* p, u32 target) {
#pragma unroll 1
  for (u32 spins = 0; spins < SPIN_LDS; ++spins) {
    if (__builtin_amdgcn_readfirstlane(*p) >= target) break;
  }
  asm volatile("" ::: "memory");
}
__device__ __forceinline__ void lds_signal(LAS u32* p) {
  asm volatile("s_waitcnt lgkmcnt(0)\n\ts_mov_b64 exec, 1\n\tds_add_u32 %0, %1\n\ts_mov_b64 exec, -1" ::"v"((u32)(uintptr_t)p), "v"(1u) : "memory");
}
__device__ __forceinline__ u32 now() { return (u32)__builtin_amdgcn_s_memrealtime(); }
__device__ __forceinline__ void hold(volatile LAS u32* tr, u32 t0, u32 tau, u32 del) {
  const u32 t1 = now();
#pragma unroll 1
  for (;;) {
    const u32 t = now();
    const u32 known = __builtin_amdgcn_readfirstlane(tr[0]), tb = __builtin_amdgcn_readfirstlane(tr[1]);
    if (t - t1 >= del && (known ? (int)(t - tb) >= (int)tau : t - t0 >= tau + TOWN)) break;
  }
}
__device__ __forceinline__ void gate(u32 t0, u32 tau) {
#pragma unroll 1
  while (now() - t0 < tau) {}
}
__device__ __forceinline__ v4u ld_sc1(__amdgpu_buffer_rsrc_t rs, u32 off) { return __builtin_amdgcn_raw_buffer_load_b128(rs, (int)off, 0, 16); }
__device__ __forceinline__ void st_sc1(__amdgpu_buffer_rsrc_t rs, u32 off, v4u v) { __builtin_amdgcn_raw_buffer_store_b128(v, rs, (int)off, 0, 16); }
__device__ __forceinline__ u32 bad32(v4u v, u32 t32) { return (v.y ^ t32) | (v.w ^ t32); }
__device__ __forceinline__ u32 bad40(v4u v, u32 t32, u32 t8) { return (v.y ^ t32) | (v.w ^ t32) | (((v.x ^ t8) | (v.z ^ t8)) & 0xffu); }
__device__ __forceinline__ u32 pk24(float v, u32 t8) { return (__float_as_uint(v) & 0xffffff00u) | t8; }
__device__ __forceinline__ float un24(u32 v) { return __uint_as_float(v & 0xffffff00u); }
__device__ __forceinline__ float fdiv(float a, float d, float rd) {
  const float q = a * rd;
  return fmaf(fmaf(-q, d, a), rd, q);
}
__device__ __forceinline__ void ln_stats(const f4 (&v)[4], float& mb, float& den, float& rd) {
  const f4 s4 = (v[0] + v[1]) + (v[2] + v[3]);
  const float mean = wave_sum((s4.x + s4.y) + (s4.z + s4.w)) * (1.f / 1024.f);
  f4 d[4];
#pragma unroll
  for (int q = 0; q < 4; ++q) { d[q] = v[q] - mean; d[q] *= d[q]; }
  const f4 q4 = (d[0] + d[1]) + (d[2] + d[3]);
  const float var = wave_sum((q4.x + q4.y) + (q4.z + q4.w)) * (1.f / 1024.f);
  mb = rbf(mean); den = rbf(__builtin_sqrtf(rbf(rbf(var) + 1e-5f))); rd = __builtin_amdgcn_rcpf(den);
}
__device__ __forceinline__ v2u ln_apply(f4 v, float mb, float den, float rd) {
#pragma unroll
  for (int z = 0; z < 4; ++z) v[z] = fdiv(rbf(v[z] - mb), den, rd);
  return v2u{pkbf(v.x, v.y), pkbf(v.z, v.w)};
}
#define PERMPAIR(bt, e, wa, wb) do { PIN2(wa, wb); \
    u32 t_[8]; \
    _Pragma("unroll") for (int d_ = 0; d_ < 4; ++d_) { t_[2 * d_] = plo(wa[d_], wb[d_]); t_[2 * d_ + 1] = phi(wa[d_], wb[d_]); } \
    asm volatile("" : "+v"(t_[0]), "+v"(t_[1]), "+v"(t_[2]), "+v"(t_[3]), "+v"(t_[4]), "+v"(t_[5]), "+v"(t_[6]), "+v"(t_[7])); \
    _Pragma("unroll") for (int d_ = 0; d_ < 8; ++d_) bt[d_][e] = t_[d_]; } while (0)
__device__ __forceinline__ void gemv_acc(const v4u (&bt)[8], v4u xa, f4 (&d)[8]) {
#pragma unroll
  for (int t = 0; t < 8; ++t) d[t] = mfma(xa, bt[t], d[t]);
}
__device__ __forceinline__ f2 gemv_fin(const f4 (&d)[8], int lane) {
  const bool set = lane & 8;
  float v[8];
#pragma unroll
  for (int t = 0; t < 8; ++t) v[t] = set ? d[t].y : d[t].x;
  const bool k0 = lane & 16, k1 = lane & 32;
  float u0 = k1 ? (k0 ? v[6] : v[4]) : (k0 ? v[2] : v[0]);
  float u1 = k1 ? (k0 ? v[7] : v[5]) : (k0 ? v[3] : v[1]);
  u0 += dpp<ROR8>(u0); u1 += dpp<ROR8>(u1);
  return f2{u0, u1};
}
__device__ __forceinline__ f2 gemv_mfma(const v4u (&bt)[8], v4u xa, int lane) {
  f4 d[8];
#pragma unroll
  for (int t = 0; t < 8; ++t) d[t] = f4{0.f, 0.f, 0.f, 0.f};
  gemv_acc(bt, xa, d);
  return gemv_fin(d, lane);
}

__global__ void __launch_bounds__(NTHR, 1) fused_decode_kernel(Params p) {
  __shared__ __attribute__((aligned(16))) u8 smem[L_BYTES];
  LAS u8* L = (LAS u8*)smem;
  volatile LAS u32* C = (volatile LAS u32*)(L + L_CTRL);
  LAS u32* CA = (LAS u32*)(L + L_CTRL);
  const u32 t0 = now();
  const int lane = threadIdx.x & 63, wave = __builtin_amdgcn_readfirstlane(threadIdx.x >> 6);
  const int bid = blockIdx.x, xcd = bid & 7, loc = bid >> 3, r = 8 * xcd + (loc >> 2), j = loc & 3;
  const int g = xcd >> 1, sp = (xcd & 1) * 16 + (loc >> 1), hh = loc & 1;
  const bool has_w1 = (loc >> 2) < 7;
  const int sl = 7 * xcd + (loc >> 2);
  const int ci = lane & 15, kg = lane >> 4;
  v4u cpf;
  asm volatile("" : "=v"(cpf));
  if (wave == 8 && loc < CODEKB) {
    const u64 pc = __builtin_amdgcn_s_getpc() & ~255ull;
    asm volatile("global_load_dwordx4 %0, %1, %2" : "=v"(cpf) : "v"(min((u32)(loc * 1024 + lane * 16), (u32)(CODE_BYTES - 16))), "s"(pc) : "memory");
  }
  const __amdgpu_buffer_rsrc_t rs = __builtin_amdgcn_make_buffer_rsrc((void*)p.ws, 0, WS_END, 0x00020000);
  asm volatile("" ::"s"(p.wq), "s"(p.x), "s"(p.ws), "s"(p.b1), "s"(p.b2), "s"(p.kc), "s"(p.vc), "s"(p.w1), "s"(p.wo), "s"(p.w2), "s"(p.out));
  const int set = ci >> 3, ch = ci & 7;
  v2u xr[4], eo;
  v4u a[8];
  asm volatile("" : "=v"(xr[0]), "=v"(xr[1]), "=v"(xr[2]), "=v"(xr[3]), "=v"(eo));
  asm volatile("" : "=v"(a[0]), "=v"(a[1]), "=v"(a[2]), "=v"(a[3]), "=v"(a[4]), "=v"(a[5]), "=v"(a[6]), "=v"(a[7]));
  u32 wv2 = wave;
  asm volatile("" : "+s"(wv2));
  if (__builtin_expect(wv2 == 0, 1)) {
#pragma unroll
    for (int q = 0; q < 4; ++q)
      asm volatile("global_load_dwordx2 %0, %1, %2" : "=v"(xr[q]) : "v"((u32)((j ^ q) * 512 + lane * 8)), "s"(p.x) : "memory");
    asm volatile("global_load_dwordx2 %0, %1, %2 sc1" : "=v"(eo) : "v"(0u), "s"(p.ws) : "memory");
    if (lane < 16) C[lane] = 0u;
    if (lane < 32) *(LAS u32*)(L + L_HIMG + lane * 4) = 0u;
    asm volatile("s_waitcnt lgkmcnt(0)" ::: "memory");
  }
  u32 wv3 = wave - 1;
  asm volatile("" : "+s"(wv3));
  if (__builtin_expect(wv3 < 4u, 1)) {
    const u8* qq = p.wq + (size_t)(256 * j + 64 * wv3 + 32 * set + 8 * kg) * 8192 + 128 * r + 16 * ch;
    __builtin_amdgcn_s_sleep(2);
#pragma unroll
    for (int i = 0; i < 8; ++i) LDNT(a[i], qq + i * 8192);
  }
  __builtin_amdgcn_s_barrier();
  __builtin_amdgcn_sched_barrier(0);
  if (wave >= 1 && wave <= 4) {
    const int w = wave - 1;
    v4u b[8], bt[8], bo[8];
    const size_t kvoff = ((size_t)g * SK + (SK - WIN) + 128 * sp) * 256;
    const u32 lbase = (u32)(uintptr_t)L;
    const u8* kp = p.kc + kvoff + (32 * w + kg) * 256;
    const u8* vp = p.vc + kvoff + w * 8192 + lane * 16;
    const u8* q1 = p.w1 + (size_t)(256 * j + 64 * w + 32 * set + 8 * kg) * 7168 + 128 * sl + 16 * ch;
    const u8* qo = p.wo + (size_t)(64 * r + 32 * set + 8 * kg) * 2048 + 512 * j + 128 * w + 16 * ch;
    const u8* q2 = p.w2 + (size_t)(56 * r + 28 * set + 7 * kg) * 2048 + 512 * j + 128 * w + 16 * ch;
    const u32 fragoff = (32 * (ci & 1) + 8 * kg) * 2;
    const u32 partoff = w * 256 + (8 * ch + 2 * kg) * 4;
#define KLD(i) dma16(kp + (i) * 1024 + ((ci ^ ((4 * (i) + kg) & 15)) << 4), lbase + L_K + w * 8192 + (i) * 1024)
#define VLD(i) dma16(vp + (i) * 1024, lbase + L_V + w * 8192 + (i) * 1024)
    waitvm<5>(); KLD(0); PERMPAIR(bt, 0, a[0], a[1]);
    waitvm<5>(); KLD(1); PERMPAIR(bt, 1, a[2], a[3]);
    waitvm<5>(); KLD(2);
    waitvm<5>(); KLD(3); PERMPAIR(bt, 2, a[4], a[5]);
    waitvm<5>(); KLD(4);
    lds_wait(C + C_LN, 1);
    v4u xfrag = *(const LAS v4u*)(L + L_XS + 128 * w + fragoff);
    const u32 t32 = __builtin_amdgcn_readfirstlane(C[C_T32]), t8 = __builtin_amdgcn_readfirstlane(C[C_T8]);
    asm volatile("" : "+v"(xfrag));
    waitvm<5>(); KLD(5); PERMPAIR(bt, 3, a[6], a[7]);
    {
      const f2 t = gemv_mfma(bt, xfrag, lane);
      if (!set) *(LAS f2*)(L + L_QPART + partoff) = t;
      lds_signal(CA + C_QP);
    }
    waitvm<11>(); KLD(6);
    waitvm<11>(); KLD(7);
#pragma unroll
    for (int i = 0; i < 8; ++i) { waitvm<11>(); VLD(i); }
    if (has_w1) {
#pragma unroll
      for (int i = 0; i < 6; ++i) { waitvm<5>(); LDNT(a[i], q1 + i * 7168); }
      lds_signal(CA + C_KV);
      waitvm<5>(); LDNT(a[6], q1 + 6 * 7168);
      waitvm<5>(); LDNT(a[7], q1 + 7 * 7168); PERMPAIR(bt, 0, a[0], a[1]);
    } else {
      waitvm<0>();
      lds_signal(CA + C_KV);
    }
    waitvm<5>(); LDNT(b[0], qo);
    waitvm<5>(); LDNT(b[1], qo + 2048); if (has_w1) PERMPAIR(bt, 1, a[2], a[3]);
    waitvm<5>(); LDNT(b[2], qo + 4096);
    waitvm<5>(); LDNT(b[3], qo + 6144); if (has_w1) PERMPAIR(bt, 2, a[4], a[5]);
    waitvm<5>(); LDNT(b[4], qo + 8192);
    waitvm<5>(); LDNT(b[5], qo + 10240);
    if (has_w1) {
      PERMPAIR(bt, 3, a[6], a[7]);
      lds_wait(C + C_LY, 1);
      const f2 t = gemv_mfma(bt, *(const LAS v4u*)(L + L_YS + 128 * w + fragoff), lane);
      if (!set) *(LAS f2*)(L + L_HPART + partoff) = t;
      lds_signal(CA + C_HP);
    }
    waitvm<5>(); LDNT(b[6], qo + 12288);
    waitvm<5>(); LDNT(b[7], qo + 14336); PERMPAIR(bo, 0, b[0], b[1]);
    waitvm<5>(); LDNT(a[0], q2);
    waitvm<5>(); LDNT(a[1], q2 + 2048); PERMPAIR(bo, 1, b[2], b[3]);
    waitvm<5>(); LDNT(a[2], q2 + 4096);
    waitvm<5>(); LDNT(a[3], q2 + 6144); PERMPAIR(bo, 2, b[4], b[5]);
    waitvm<5>(); LDNT(a[4], q2 + 8192);
    waitvm<5>(); LDNT(a[5], q2 + 10240); PERMPAIR(bo, 3, b[6], b[7]);
    waitvm<5>(); LDNT(a[6], q2 + 12288);
    waitvm<5>(); PERMPAIR(bt, 0, a[0], a[1]);
    waitvm<3>(); PERMPAIR(bt, 1, a[2], a[3]);
    waitvm<1>(); PERMPAIR(bt, 2, a[4], a[5]);
    waitvm<0>();
    a[7] = v4u{0u, 0u, 0u, 0u};
    PERMPAIR(bt, 3, a[6], a[7]);
    f4 d[8];
#pragma unroll
    for (int t = 0; t < 8; ++t) d[t] = f4{0.f, 0.f, 0.f, 0.f};
    lds_wait(C + C_H, 1);
    gemv_acc(bt, *(const LAS v4u*)(L + L_HIMG + fragoff), d);
    lds_wait(C + C_O, 4);
    gemv_acc(bo, *(const LAS v4u*)(L + L_OIMG + fragoff), d);
    const f2 t = gemv_fin(d, lane);
    if (!set) st_sc1(rs, WS_FP + (r * 1024 + 256 * j + 64 * w + 8 * ch + 2 * kg) * 8, v4u{pk24(t.x, t8), t32, pk24(t.y, t8), t32});
    lds_signal(CA + C_FA);
  } else if (wave >= 5) {
    const int ag = wave - 5;
    constexpr float LOG2E = 1.4426950408889634f;
    lds_wait(C + C_LN, 1);
    const u32 t32 = __builtin_amdgcn_readfirstlane(C[C_T32]), t8 = __builtin_amdgcn_readfirstlane(C[C_T8]);
    asm volatile("s_waitcnt vmcnt(0)" : "+v"(cpf)::"memory");
    lds_wait(C + C_QP, 4);
    if (ag == 1 && lane < 32) {
      const LAS f2* pp = (const LAS f2*)(L + L_QPART + lane * 8);
      const f2 u = (pp[0] + pp[32]) + (pp[64] + pp[96]);
      st_sc1(rs, WS_QP + (j * 4096 + 64 * r + 2 * lane) * 8, v4u{__float_as_uint(u.x), t32, __float_as_uint(u.y), t32});
    }
    v4u kf[2][4], vf[4][2];
    const int hd = 8 * g + 4 * hh + kg;
    u32 qoff = L_QIMG + (ci >> 2) * 272 + 16 * kg, mloff = L_MLX + (ci >> 2) * 32 + ag * 8;
    u32 opoff = WS_OP + (((2 * hd + (ag >> 1)) * 32 + sp) * 32 + 16 * (ag & 1) + ci) * 8, mlst = WS_ML + (hd * 32 + sp) * 16;
    {
      v4u v[4];
      const u32 o0 = WS_QP + (1024 * g + 512 * hh + 128 * ag + 2 * lane) * 8;
      hold(C + C_TR, t0, TAU1, 30);
#pragma unroll
      for (int k = 0; k < 4; ++k) v[k] = ld_sc1(rs, o0 + k * 32768);
      __builtin_amdgcn_sched_barrier(0);
      lds_wait(C + C_KV, 4);
#pragma unroll
      for (int T = 0; T < 2; ++T)
#pragma unroll
        for (int t = 0; t < 4; ++t) kf[T][t] = *(const LAS v4u*)(L + L_K + (32 * ag + 16 * T + ci) * 256 + (((4 * t + kg) ^ ci) << 4));
#pragma unroll
      for (int u = 0; u < 4; ++u) {
        u32 vv[8];
#pragma unroll
        for (int e = 0; e < 8; ++e) vv[e] = *(const LAS u32*)(L + L_V + (32 * u + 16 * (e >> 2) + 4 * kg + (e & 3)) * 256 + 64 * ag + 4 * ci);
#pragma unroll
        for (int e = 0; e < 4; ++e) { vf[u][0][e] = plo(vv[2 * e], vv[2 * e + 1]); vf[u][1][e] = phi(vv[2 * e], vv[2 * e + 1]); }
        PIN2(vf[u][0], vf[u][1]);
      }
      asm volatile("" : "+v"(qoff), "+v"(mloff), "+v"(opoff), "+v"(mlst));
      PIN4(kf[0][0], kf[0][1], kf[0][2], kf[0][3]);
      PIN4(kf[1][0], kf[1][1], kf[1][2], kf[1][3]);
      __builtin_amdgcn_sched_barrier(0);
      u32 spins = 0, bad = bad32(v[0], t32) | bad32(v[1], t32) | bad32(v[2], t32) | bad32(v[3], t32);
#pragma unroll 1
      while (bad != 0 && ++spins < SPIN_GLB) {
        __builtin_amdgcn_s_sleep(1);
#pragma unroll
        for (int k = 0; k < 4; ++k) v[k] = ld_sc1(rs, o0 + k * 32768);
        bad = bad32(v[0], t32) | bad32(v[1], t32) | bad32(v[2], t32) | bad32(v[3], t32);
        asm volatile("" ::: "memory");
      }
      const float q0 = (__uint_as_float(v[0].x) + __uint_as_float(v[1].x)) + (__uint_as_float(v[2].x) + __uint_as_float(v[3].x));
      const float q1 = (__uint_as_float(v[0].z) + __uint_as_float(v[1].z)) + (__uint_as_float(v[2].z) + __uint_as_float(v[3].z));
      *(LAS u32*)(L + L_QIMG + ag * 272 + lane * 4) = pkbf(q0, q1);
      lds_signal(CA + C_Q);
    }
    lds_wait(C + C_Q, 4);
    {
      v4u qf[4];
#pragma unroll
      for (int t = 0; t < 4; ++t) qf[t] = *(const LAS v4u*)(L + qoff + 64 * t);
      PIN4(qf[0], qf[1], qf[2], qf[3]);
      f4 s0 = {0.f, 0.f, 0.f, 0.f}, s1 = {0.f, 0.f, 0.f, 0.f};
#pragma unroll
      for (int t = 0; t < 4; ++t) { s0 = mfma(kf[0][t], qf[t], s0); s1 = mfma(kf[1][t], qf[t], s1); }
      constexpr float SC = 0.08838834764831845f;
      s0 = rbf4(s0) * SC; s1 = rbf4(s1) * SC;
      const float m = rbf(max32(max16(fmaxf(fmaxf(fmaxf(s0.x, s0.y), fmaxf(s0.z, s0.w)), fmaxf(fmaxf(s1.x, s1.y), fmaxf(s1.z, s1.w))))));
      s0 = (rbf4(s0) - m) * LOG2E; s1 = (rbf4(s1) - m) * LOG2E;
#pragma unroll
      for (int e = 0; e < 4; ++e) { s0[e] = __builtin_amdgcn_exp2f(s0[e]); s1[e] = __builtin_amdgcn_exp2f(s1[e]); }
      *(LAS v4u*)(L + L_PX + ag * 1024 + lane * 16) = v4u{cvtpk(s0.x, s0.y), cvtpk(s0.z, s0.w), cvtpk(s1.x, s1.y), cvtpk(s1.z, s1.w)};
      const f4 ss = s0 + s1;
      const float l = xrow_sum((ss.x + ss.y) + (ss.z + ss.w));
      if ((lane & 0x33) == 0) *(LAS f2*)(L + mloff) = f2{m, l};
      lds_signal(CA + C_S);
    }
    lds_wait(C + C_S, 4);
    {
      v4u px[4];
      const f4 ml01 = *(const LAS f4*)(L + L_MLX + kg * 32), ml23 = *(const LAS f4*)(L + L_MLX + kg * 32 + 16);
#pragma unroll
      for (int u = 0; u < 4; ++u) px[u] = *(const LAS v4u*)(L + L_PX + u * 1024 + lane * 16);
      PIN4(px[0], px[1], px[2], px[3]);
      const f4 z = {0.f, 0.f, 0.f, 0.f};
      float d0[4], d1[4];
#pragma unroll
      for (int u = 0; u < 4; ++u) { d0[u] = mfma(px[u], vf[u][0], z).x; d1[u] = mfma(px[u], vf[u][1], z).x; }
      const float m = fmaxf(fmaxf(ml01.x, ml01.z), fmaxf(ml23.x, ml23.z));
      const float f0 = __builtin_amdgcn_exp2f((ml01.x - m) * LOG2E), f1 = __builtin_amdgcn_exp2f((ml01.z - m) * LOG2E);
      const float f2_ = __builtin_amdgcn_exp2f((ml23.x - m) * LOG2E), f3 = __builtin_amdgcn_exp2f((ml23.z - m) * LOG2E);
      const float o0 = (f0 * d0[0] + f1 * d0[1]) + (f2_ * d0[2] + f3 * d0[3]);
      const float o1 = (f0 * d1[0] + f1 * d1[1]) + (f2_ * d1[2] + f3 * d1[3]);
      const u32 po = cvtpk(o0, o1), pn = __builtin_amdgcn_mov_dpp(po, XOR1, 0xf, 0xf, true);
      if (!(ci & 1)) st_sc1(rs, opoff, v4u{po, t32, pn, t32});
      if (ag == 0) {
        const float lsum = (f0 * ml01.y + f1 * ml01.w) + (f2_ * ml23.y + f3 * ml23.w);
        if (ci == 0) st_sc1(rs, mlst, v4u{pk24(m, t8), t32, pk24(lsum, t8), t32});
      }
    }
    {
      v4u v0, v1, ml;
      const int sg = lane >> 2;
      const u32 o0 = WS_OP + ((r * 32 + sg) * 32 + 8 * ag + 2 * (lane & 3)) * 8;
      const u32 om = WS_ML + ((r >> 1) * 32 + (lane & 31)) * 16;
      u32 spins = 0, bad;
      hold(C + C_TR, t0, TAU2, 30);
#pragma unroll 1
      do {
        if (spins) __builtin_amdgcn_s_sleep(1);
        ml = ld_sc1(rs, om);
        v0 = ld_sc1(rs, o0); v1 = ld_sc1(rs, o0 + 4096);
        bad = bad40(ml, t32, t8) | bad32(v0, t32) | bad32(v1, t32);
        asm volatile("" ::: "memory");
      } while (bad != 0 && ++spins < SPIN_GLB);
      const float ms = un24(ml.x), ls = un24(ml.z);
      const float M = max16(row_max(ms));
      const float ws_ = __builtin_amdgcn_exp2f((ms - M) * LOG2E);
      const float f0 = __shfl(ws_, sg), f1 = __shfl(ws_, 16 + sg);
      float lt = row_sum(ws_ * ls);
      lt = tr16(lt, lt);
      const float inv = __builtin_amdgcn_rcpf(lt);
      const f4 e4 = unpk4(v2u{v0.x, v0.z}) * f0 + unpk4(v2u{v1.x, v1.z}) * f1;
      float e = tr16(tr32(e4.x, e4.z), tr32(e4.y, e4.w));
      e += dpp<ROR8>(e);
      e += dpp<ROR4>(e);
      e *= inv;
      if ((lane & 12) == 0) *(LAS unsigned short*)(L + L_OIMG + (16 * ag + 4 * (lane & 3) + 2 * (lane >> 5) + (kg & 1)) * 2) = (unsigned short)pkbf(e, e);
      lds_signal(CA + C_O);
    }
  } else {
    asm volatile("s_waitcnt vmcnt(0)" : "+v"(xr[0]), "+v"(xr[1]), "+v"(xr[2]), "+v"(xr[3]), "+v"(eo)::"memory");
    const u64 e = (((u64)eo.y << 32) | eo.x) + 1;
    const u32 t32 = __builtin_amdgcn_readfirstlane((u32)e), t8 = __builtin_amdgcn_readfirstlane((u32)(e >> 32)) & 0xffu;
    {
      f4 v[4] = {unpk4(xr[0]), unpk4(xr[1]), unpk4(xr[2]), unpk4(xr[3])};
      float mb, den, rd;
      if (lane == 0) {
        C[C_T32] = t32; C[C_T8] = t8;
        if (loc == 0) { const u32 tx = now(); st_sc1(rs, WS_ST + xcd * 16, v4u{tx, t32, tx, t32}); }
      }
      ln_stats(v, mb, den, rd);
      const v2u x0 = ln_apply(v[0], mb, den, rd);
      *(LAS v2u*)(L + L_XS + lane * 8) = x0;
      lds_signal(CA + C_LN);
      __builtin_amdgcn_sched_barrier(0);
      v[0] = unpk4(x0);
#pragma unroll
      for (int q = 1; q < 4; ++q) v[q] = unpk4(ln_apply(v[q], mb, den, rd));
      ln_stats(v, mb, den, rd);
      *(LAS v2u*)(L + L_YS + lane * 8) = ln_apply(v[0], mb, den, rd);
      lds_signal(CA + C_LY);
    }
    {
      gate(t0, TRD);
      v4u z0;
      u32 spins = 0, bad;
#pragma unroll 1
      do {
        if (spins) __builtin_amdgcn_s_sleep(10);
        z0 = ld_sc1(rs, WS_ST + (lane & 7) * 16);
        bad = bad32(z0, t32);
        asm volatile("" ::: "memory");
      } while (bad != 0 && ++spins < SPIN_GLB);
      const u32 tb = t0 + (u32)(int)fminf(row_max((float)(int)(z0.x - t0)), 1000.f);
      if (lane == 0) C[C_TRV] = tb;
      lds_signal(CA + C_TR);
    }
    const int li = (lane & 31) < 28 ? (lane & 31) : 27;
    const int cg = 32 * xcd + loc;
    const u32 b1w = *(const GAS u32*)(p.b1 + (56 * r + 2 * li) * 2);
    const u32 xfin = *(const GAS unsigned short*)(p.x + (4 * cg + kg) * 2), bfin = *(const GAS unsigned short*)(p.b2 + (4 * cg + kg) * 2);
    if (has_w1) {
      lds_wait(C + C_HP, 4);
      if (lane < 32) {
        const LAS f2* pp = (const LAS f2*)(L + L_HPART + lane * 8);
        const f2 u = (pp[0] + pp[32]) + (pp[64] + pp[96]);
        st_sc1(rs, WS_HP + (j * 3584 + 64 * sl + 2 * lane) * 8, v4u{__float_as_uint(u.x), t32, __float_as_uint(u.y), t32});
      }
    }
    {
      const u32 o0 = WS_HP + ((lane >> 5) * 3584 + 56 * r + 2 * li) * 8;
      v4u v0, v1;
      u32 spins = 0, bad;
      hold(C + C_TR, t0, TAUH, 40);
#pragma unroll 1
      do {
        if (spins) __builtin_amdgcn_s_sleep(1);
        v0 = ld_sc1(rs, o0); v1 = ld_sc1(rs, o0 + 2 * 3584 * 8);
        bad = bad32(v0, t32) | bad32(v1, t32);
        asm volatile("" ::: "memory");
      } while (bad != 0 && ++spins < SPIN_GLB);
      float h0 = __uint_as_float(v0.x) + __uint_as_float(v1.x), h1 = __uint_as_float(v0.z) + __uint_as_float(v1.z);
      h0 = tr32(h0, h0); h1 = tr32(h1, h1);
      const float hv[2] = {rbf(rbf(h0) + bf2f(b1w & 0xffffu)), rbf(rbf(h1) + __uint_as_float(b1w & 0xffff0000u))};
#pragma unroll
      for (int z = 0; z < 2; ++z) {
        const float t = hv[z], u = 0.7978845608028654f * (t + 0.044715f * t * t * t);
        const float th = 1.f - 2.f / (__expf(2.f * u) + 1.f);
        const int n = 2 * lane + z, st = n >= 28, nn = n - 28 * st, k7 = nn / 7;
        if (lane < 28) *(LAS unsigned short*)(L + L_HIMG + (32 * st + 8 * k7 + (nn - 7 * k7)) * 2) = (unsigned short)pkbf(0.5f * t * (1.f + th), 0.f);
      }
      lds_signal(CA + C_H);
    }
    {
      v4u v0, v1;
      const u32 o0 = WS_FP + (lane * 1024 + 4 * cg) * 8;
      lds_wait(C + C_FA, 4);
      gate(now(), 20);
      u32 spins = 0, bad;
#pragma unroll 1
      do {
        if (spins) __builtin_amdgcn_s_sleep(1);
        v0 = ld_sc1(rs, o0); v1 = ld_sc1(rs, o0 + 16);
        bad = bad40(v0, t32, t8) | bad40(v1, t32, t8);
        asm volatile("" ::: "memory");
      } while (bad != 0 && ++spins < SPIN_GLB);
      const float pa = row_sum(tr16(tr32(un24(v0.x), un24(v1.x)), tr32(un24(v0.z), un24(v1.z))));
      const float res = (bf2f(xfin) + bf2f(bfin)) + pa;
      if (ci == 0) *(GAS unsigned short*)(p.out + (4 * cg + kg) * 2) = (unsigned short)(pkbf(res, res) & 0xffffu);
    }
    if (bid == 0 && lane == 0)
      __hip_atomic_store((GAS u64*)p.ws, e, __ATOMIC_RELAXED, __HIP_MEMORY_SCOPE_AGENT);
  }
}
}

extern "C" void kernel_launch(void* const* d_in, const int* in_sizes, int n_in, void* d_out, int out_size, void* d_ws, size_t ws_size,
                              hipStream_t stream) {
  Params p;
  p.x = (const u8*)d_in[0]; p.kc = (const u8*)d_in[1]; p.vc = (const u8*)d_in[2]; p.wq = (const u8*)d_in[3]; p.wo = (const u8*)d_in[4];
  p.w1 = (const u8*)d_in[6]; p.b1 = (const u8*)d_in[7]; p.w2 = (const u8*)d_in[8]; p.b2 = (const u8*)d_in[9];
  p.out = (u8*)d_out; p.ws = (u8*)d_ws;
  hipLaunchKernelGGL(fused_decode_kernel, dim3(256), dim3(NTHR), 0, stream, p);
}
```

```cpp
#include <hip/hip_runtime.h>
#include <stdint.h>

#define GAS __attribute__((address_space(1)))
#define LAS __attribute__((address_space(3)))
typedef unsigned u32;
typedef unsigned long long u64;
typedef unsigned char u8;
typedef u32 v4u __attribute__((ext_vector_type(4)));
typedef u32 v2u __attribute__((ext_vector_type(2)));
typedef float f4 __attribute__((ext_vector_type(4)));
typedef float f2 __attribute__((ext_vector_type(2)));
typedef short bf16x8 __attribute__((ext_vector_type(8)));
typedef __bf16 bf16x2 __attribute__((ext_vector_type(2)));

namespace {
#define TAU1 310
#define TAU2 640
#define TAUH 582
#define TOWN 140
#define TRD 220
#define CODEKB 16
#define CODE_BYTES 16384

constexpr int SK = 65536, WIN = 4096;
constexpr int NTHR = 9 * 64;
constexpr u32 WS_QP = 4096;
constexpr u32 WS_HP = WS_QP + 4 * 4096 * 8;
constexpr u32 WS_ML = WS_HP + 4 * 3584 * 8;
constexpr u32 WS_OP = WS_ML + 32 * 32 * 16;
constexpr u32 WS_FP = WS_OP + 64 * 32 * 32 * 8;
constexpr u32 WS_ST = WS_FP + 64 * 1024 * 8;
constexpr u32 WS_END = WS_ST + 128;
constexpr int L_K = 0, L_V = 32768, L_XS = 65536, L_YS = L_XS + 512, L_QPART = L_YS + 512, L_HPART = L_QPART + 1024,
              L_QIMG = L_HPART + 1024, L_PX = L_QIMG + 1152, L_MLX = L_PX + 4096, L_OIMG = L_MLX + 128, L_HIMG = L_OIMG + 128,
              L_CTRL = L_HIMG + 128, L_USED = L_CTRL + 128, L_BYTES = 90112;
static_assert(L_USED <= L_BYTES, "lds");
enum { C_LN = 0, C_LY, C_QP, C_KV, C_HP, C_Q, C_S, C_O, C_H, C_FA, C_T32, C_T8, C_TR, C_TRV };
constexpr u32 SPIN_LDS = 1u << 22, SPIN_GLB = 1u << 18;

struct Params { const u8 *wq, *x; u8* ws; const u8 *b1, *b2, *kc, *vc, *w1, *wo, *w2; u8* out; };

__device__ __forceinline__ float bf2f(u32 b) { return __uint_as_float(b << 16); }
__device__ __forceinline__ u32 pkbf(float lo, float hi) {
  bf16x2 v = {(__bf16)lo, (__bf16)hi};
  return __builtin_bit_cast(u32, v);
}
__device__ __forceinline__ float rbf(float f) { return (float)(__bf16)f; }
__device__ __forceinline__ f4 unpk4(v2u x) {
  return f4{bf2f(x.x & 0xffffu), __uint_as_float(x.x & 0xffff0000u), bf2f(x.y & 0xffffu), __uint_as_float(x.y & 0xffff0000u)};
}
__device__ __forceinline__ u32 cvtpk(float lo, float hi) { u32 r = pkbf(lo, hi); asm("" : "+v"(r)); return r; }
__device__ __forceinline__ f4 rbf4(f4 v) { return unpk4(v2u{cvtpk(v.x, v.y), cvtpk(v.z, v.w)}); }
__device__ __forceinline__ u32 plo(u32 a, u32 b) { return __builtin_amdgcn_perm(b, a, 0x05040100u); }
__device__ __forceinline__ u32 phi(u32 a, u32 b) { return __builtin_amdgcn_perm(b, a, 0x07060302u); }
__device__ __forceinline__ f4 mfma(v4u a, v4u b, f4 c) {
  return __builtin_amdgcn_mfma_f32_16x16x32_bf16(__builtin_bit_cast(bf16x8, a), __builtin_bit_cast(bf16x8, b), c, 0, 0, 0);
}
template <int CTRL> __device__ __forceinline__ float dpp(float x) {
  return __builtin_bit_cast(float, __builtin_amdgcn_mov_dpp(__builtin_bit_cast(int, x), CTRL, 0xf, 0xf, true));
}
constexpr int XOR1 = 0xB1, XOR2 = 0x4E, HMIR = 0x141, ROR8 = 0x128, ROR4 = 0x124;
__device__ __forceinline__ float tr32(float a, float b) {
  auto s = __builtin_amdgcn_permlane32_swap(__float_as_uint(a), __float_as_uint(b), false, false);
  return __uint_as_float(s[0]) + __uint_as_float(s[1]);
}
__device__ __forceinline__ float tr16(float a, float b) {
  auto s = __builtin_amdgcn_permlane16_swap(__float_as_uint(a), __float_as_uint(b), false, false);
  return __uint_as_float(s[0]) + __uint_as_float(s[1]);
}
__device__ __forceinline__ float max16(float x) {
  auto s = __builtin_amdgcn_permlane16_swap(__float_as_uint(x), __float_as_uint(x), false, false);
  return fmaxf(__uint_as_float(s[0]), __uint_as_float(s[1]));
}
__device__ __forceinline__ float max32(float x) {
  auto s = __builtin_amdgcn_permlane32_swap(__float_as_uint(x), __float_as_uint(x), false, false);
  return fmaxf(__uint_as_float(s[0]), __uint_as_float(s[1]));
}
__device__ __forceinline__ float row_sum(float v) {
  v += dpp<XOR1>(v); v += dpp<XOR2>(v); v += dpp<HMIR>(v); v += dpp<ROR8>(v);
  return v;
}
__device__ __forceinline__ float row_max(float v) {
  v = fmaxf(v, dpp<XOR1>(v)); v = fmaxf(v, dpp<XOR2>(v)); v = fmaxf(v, dpp<HMIR>(v)); v = fmaxf(v, dpp<ROR8>(v));
  return v;
}
__device__ __forceinline__ float xrow_sum(float x) { x = tr16(x, x); return tr32(x, x); }
__device__ __forceinline__ float wave_sum(float v) { return xrow_sum(row_sum(v)); }

#define LDNT(dst, ptr) asm volatile("global_load_dwordx4 %0, %1, off nt" : "=v"(dst) : "v"(ptr) : "memory")
#define PIN2(a, b) asm volatile("" : "+v"(a), "+v"(b)::"memory")
#define PIN4(a, b, c, d) asm volatile("" : "+v"(a), "+v"(b), "+v"(c), "+v"(d)::"memory")
template <int N> __device__ __forceinline__ void waitvm() { asm volatile("s_waitcnt vmcnt(%0)" ::"i"(N) : "memory"); }
__device__ __forceinline__ void dma16(const u8* src, u32 lds_dst) {
  u32 keep;
  asm volatile("s_mov_b32 %0, m0\n\ts_mov_b32 m0, %2\n\ts_nop 0\n\tglobal_load_lds_dwordx4 %1, off nt\n\ts_mov_b32 m0, %0"
               : "=&s"(keep) : "v"(src), "s"(lds_dst) : "memory");
}
__device__ __forceinline__ void lds_wait(volatile LAS u32* p, u32 target) {
#pragma unroll 1
  for (u32 spins = 0; spins < SPIN_LDS; ++spins) {
    if (__builtin_amdgcn_readfirstlane(*p) >= target) break;
  }
  asm volatile("" ::: "memory");
}
__device__ __forceinline__ void lds_signal(LAS u32* p) {
  asm volatile("s_waitcnt lgkmcnt(0)\n\ts_mov_b64 exec, 1\n\tds_add_u32 %0, %1\n\ts_mov_b64 exec, -1" ::"v"((u32)(uintptr_t)p), "v"(1u) : "memory");
}
__device__ __forceinline__ u32 now() { return (u32)__builtin_amdgcn_s_memrealtime(); }
__device__ __forceinline__ void hold(volatile LAS u32* tr, u32 t0, u32 tau, u32 del) {
  const u32 t1 = now();
#pragma unroll 1
  for (;;) {
    const u32 t = now();
    const u32 known = __builtin_amdgcn_readfirstlane(tr[0]), tb = __builtin_amdgcn_readfirstlane(tr[1]);
    if (t - t1 >= del && (known ? (int)(t - tb) >= (int)tau : t - t0 >= tau + TOWN)) break;
  }
}
__device__ __forceinline__ void gate(u32 t0, u32 tau) {
#pragma unroll 1
  while (now() - t0 < tau) {}
}
__device__ __forceinline__ v4u ld_sc1(__amdgpu_buffer_rsrc_t rs, u32 off) { return __builtin_amdgcn_raw_buffer_load_b128(rs, (int)off, 0, 16); }
__device__ __forceinline__ void st_sc1(__amdgpu_buffer_rsrc_t rs, u32 off, v4u v) { __builtin_amdgcn_raw_buffer_store_b128(v, rs, (int)off, 0, 16); }
__device__ __forceinline__ void st_at(u8* p, u32 lo, u32 hi) {
  asm volatile("global_atomic_swap_x2 %0, %1, off sc1\n\ts_nop 1" ::"v"(p), "v"(v2u{lo, hi}) : "memory");
}
__device__ __forceinline__ u32 bad32(v4u v, u32 t32) { return (v.y ^ t32) | (v.w ^ t32); }
__device__ __forceinline__ u32 bad40(v4u v, u32 t32, u32 t8) { return (v.y ^ t32) | (v.w ^ t32) | (((v.x ^ t8) | (v.z ^ t8)) & 0xffu); }
__device__ __forceinline__ u32 pk24(float v, u32 t8) { return (__float_as_uint(v) & 0xffffff00u) | t8; }
__device__ __forceinline__ float un24(u32 v) { return __uint_as_float(v & 0xffffff00u); }
__device__ __forceinline__ float fdiv(float a, float d, float rd) {
  const float q = a * rd;
  return fmaf(fmaf(-q, d, a), rd, q);
}
__device__ __forceinline__ void ln_stats(const f4 (&v)[4], float& mb, float& den, float& rd) {
  const f4 s4 = (v[0] + v[1]) + (v[2] + v[3]);
  const float mean = wave_sum((s4.x + s4.y) + (s4.z + s4.w)) * (1.f / 1024.f);
  f4 d[4];
#pragma unroll
  for (int q = 0; q < 4; ++q) { d[q] = v[q] - mean; d[q] *= d[q]; }
  const f4 q4 = (d[0] + d[1]) + (d[2] + d[3]);
  const float var = wave_sum((q4.x + q4.y) + (q4.z + q4.w)) * (1.f / 1024.f);
  mb = rbf(mean); den = rbf(__builtin_sqrtf(rbf(rbf(var) + 1e-5f))); rd = __builtin_amdgcn_rcpf(den);
}
__device__ __forceinline__ v2u ln_apply(f4 v, float mb, float den, float rd) {
#pragma unroll
  for (int z = 0; z < 4; ++z) v[z] = fdiv(rbf(v[z] - mb), den, rd);
  return v2u{pkbf(v.x, v.y), pkbf(v.z, v.w)};
}
#define PERMPAIR(bt, e, wa, wb) do { PIN2(wa, wb); \
    u32 t_[8]; \
    _Pragma("unroll") for (int d_ = 0; d_ < 4; ++d_) { t_[2 * d_] = plo(wa[d_], wb[d_]); t_[2 * d_ + 1] = phi(wa[d_], wb[d_]); } \
    asm volatile("" : "+v"(t_[0]), "+v"(t_[1]), "+v"(t_[2]), "+v"(t_[3]), "+v"(t_[4]), "+v"(t_[5]), "+v"(t_[6]), "+v"(t_[7])); \
    asm volatile("" : "=v"(wa), "=v"(wb));     \
    _Pragma("unroll") for (int d_ = 0; d_ < 8; ++d_) bt[d_][e] = t_[d_]; } while (0)
__device__ __forceinline__ void gemv_acc(const v4u (&bt)[8], v4u xa, f4 (&d)[8]) {
#pragma unroll
  for (int t = 0; t < 8; ++t) d[t] = mfma(xa, bt[t], d[t]);
}
__device__ __forceinline__ f2 gemv_fin(const f4 (&d)[8], int lane) {
  const bool set = lane & 8;
  float v[8];
#pragma unroll
  for (int t = 0; t < 8; ++t) v[t] = set ? d[t].y : d[t].x;
  const bool k0 = lane & 16, k1 = lane & 32;
  float u0 = k1 ? (k0 ? v[6] : v[4]) : (k0 ? v[2] : v[0]);
  float u1 = k1 ? (k0 ? v[7] : v[5]) : (k0 ? v[3] : v[1]);
  u0 += dpp<ROR8>(u0); u1 += dpp<ROR8>(u1);
  return f2{u0, u1};
}
__device__ __forceinline__ f2 gemv_mfma(const v4u (&bt)[8], v4u xa, int lane) {
  f4 d[8];
#pragma unroll
  for (int t = 0; t < 8; ++t) d[t] = f4{0.f, 0.f, 0.f, 0.f};
  gemv_acc(bt, xa, d);
  return gemv_fin(d, lane);
}

__global__ void __launch_bounds__(NTHR, 1) fused_decode_kernel(Params p) {
  __shared__ __attribute__((aligned(16))) u8 smem[L_BYTES];
  LAS u8* L = (LAS u8*)smem;
  volatile LAS u32* C = (volatile LAS u32*)(L + L_CTRL);
  LAS u32* CA = (LAS u32*)(L + L_CTRL);
  const u32 t0 = now();
  const int lane = threadIdx.x & 63, wave = __builtin_amdgcn_readfirstlane(threadIdx.x >> 6);
  const int bid = blockIdx.x, xcd = bid & 7, loc = bid >> 3, r = 8 * xcd + (loc >> 2), j = loc & 3;
  const int g = xcd >> 1, sp = (xcd & 1) * 16 + (loc >> 1), hh = loc & 1;
  const bool has_w1 = (loc >> 2) != 3;
  const int sl = 7 * xcd + (loc >> 2) - ((loc >> 2) > 3 ? 1 : 0);
  const int ci = lane & 15, kg = lane >> 4;
  v4u cpf;
  asm volatile("" : "=v"(cpf));
  if (wave == 8 && loc < CODEKB) {
    const u64 pc = __builtin_amdgcn_s_getpc() & ~255ull;
    asm volatile("global_load_dwordx4 %0, %1, %2" : "=v"(cpf) : "v"(min((u32)(loc * 1024 + lane * 16), (u32)(CODE_BYTES - 16))), "s"(pc) : "memory");
  }
  const __amdgpu_buffer_rsrc_t rs = __builtin_amdgcn_make_buffer_rsrc((void*)p.ws, 0, WS_END, 0x00020000);
  asm volatile("" ::"s"(p.wq), "s"(p.x), "s"(p.ws), "s"(p.b1), "s"(p.b2), "s"(p.kc), "s"(p.vc), "s"(p.w1), "s"(p.wo), "s"(p.w2), "s"(p.out));
  const int set = ci >> 3, ch = ci & 7;
  v2u xr[4], eo;
  v4u a[8];
  asm volatile("" : "=v"(xr[0]), "=v"(xr[1]), "=v"(xr[2]), "=v"(xr[3]), "=v"(eo));
  asm volatile("" : "=v"(a[0]), "=v"(a[1]), "=v"(a[2]), "=v"(a[3]), "=v"(a[4]), "=v"(a[5]), "=v"(a[6]), "=v"(a[7]));
  u32 wv2 = wave;
  asm volatile("" : "+s"(wv2));
  if (__builtin_expect(wv2 == 0, 1)) {
#pragma unroll
    for (int q = 0; q < 4; ++q)
      asm volatile("global_load_dwordx2 %0, %1, %2" : "=v"(xr[q]) : "v"((u32)((j ^ q) * 512 + lane * 8)), "s"(p.x) : "memory");
    asm volatile("global_load_dwordx2 %0, %1, %2 sc1" : "=v"(eo) : "v"(0u), "s"(p.ws) : "memory");
    if (lane < 16) C[lane] = 0u;
    if (lane < 32) *(LAS u32*)(L + L_HIMG + lane * 4) = 0u;
    asm volatile("s_waitcnt lgkmcnt(0)" ::: "memory");
  }
  u32 wv3 = wave - 1;
  asm volatile("" : "+s"(wv3));
  if (__builtin_expect(wv3 < 4u, 1)) {
    const u8* qq = p.wq + (size_t)(256 * j + 64 * wv3 + 32 * set + 8 * kg) * 8192 + 128 * r + 16 * ch;
    __builtin_amdgcn_s_sleep(2);
#pragma unroll
    for (int i = 0; i < 8; ++i) LDNT(a[i], qq + i * 8192);
  }
  __builtin_amdgcn_s_barrier();
  __builtin_amdgcn_sched_barrier(0);
  if (wave >= 1 && wave <= 4) {
    const int w = wave - 1;
    v4u b[8], bt[8], bo[8];
    const size_t kvoff = ((size_t)g * SK + (SK - WIN) + 128 * sp) * 256;
    const u32 lbase = (u32)(uintptr_t)L;
    const u8* kp = p.kc + kvoff + (32 * w + kg) * 256;
    const u8* vp = p.vc + kvoff + w * 8192 + lane * 16;
    const u8* q1 = p.w1 + (size_t)(256 * j + 64 * w + 32 * set + 8 * kg) * 7168 + 128 * sl + 16 * ch;
    const u8* qo = p.wo + (size_t)(64 * r + 32 * set + 8 * kg) * 2048 + 512 * j + 128 * w + 16 * ch;
    const u8* q2 = p.w2 + (size_t)(56 * r + 28 * set + 7 * kg) * 2048 + 512 * j + 128 * w + 16 * ch;
    const u32 fragoff = (32 * (ci & 1) + 8 * kg) * 2;
    const u32 partoff = w * 256 + (8 * ch + 2 * kg) * 4;
    const u8* ks[8];
#pragma unroll
    for (int i = 0; i < 8; ++i) { ks[i] = kp + i * 1024 + ((ci ^ ((4 * i + kg) & 15)) << 4); asm volatile("" : "+v"(ks[i])); }
#define KLD(i) dma16(ks[i], lbase + L_K + w * 8192 + (i) * 1024)
#define VLD(i) dma16(vp + (i) * 1024, lbase + L_V + w * 8192 + (i) * 1024)
    waitvm<5>(); KLD(0); PERMPAIR(bt, 0, a[0], a[1]);
    waitvm<5>(); KLD(1); PERMPAIR(bt, 1, a[2], a[3]);
    waitvm<5>(); KLD(2);
    waitvm<5>(); KLD(3); PERMPAIR(bt, 2, a[4], a[5]);
    waitvm<5>(); KLD(4);
    lds_wait(C + C_LN, 1);
    v4u xfrag = *(const LAS v4u*)(L + L_XS + 128 * w + fragoff);
    const u32 t32 = __builtin_amdgcn_readfirstlane(C[C_T32]), t8 = __builtin_amdgcn_readfirstlane(C[C_T8]);
    asm volatile("" : "+v"(xfrag));
    waitvm<5>(); KLD(5); PERMPAIR(bt, 3, a[6], a[7]);
    {
      const f2 t = gemv_mfma(bt, xfrag, lane);
      if (!set) *(LAS f2*)(L + L_QPART + partoff) = t;
      lds_signal(CA + C_QP);
    }
    waitvm<11>(); KLD(6);
    waitvm<11>(); KLD(7);
#pragma unroll
    for (int i = 0; i < 8; ++i) { waitvm<11>(); VLD(i); }
    if (has_w1) {
#pragma unroll
      for (int i = 0; i < 6; ++i) { waitvm<5>(); LDNT(a[i], q1 + i * 7168); }
      lds_signal(CA + C_KV);
      waitvm<5>(); LDNT(a[6], q1 + 6 * 7168);
      waitvm<5>(); LDNT(a[7], q1 + 7 * 7168); PERMPAIR(bt, 0, a[0], a[1]);
    } else {
      waitvm<0>();
      lds_signal(CA + C_KV);
    }
    waitvm<5>(); LDNT(b[0], qo);
    waitvm<5>(); LDNT(b[1], qo + 2048); if (has_w1) PERMPAIR(bt, 1, a[2], a[3]);
    waitvm<5>(); LDNT(b[2], qo + 4096);
    waitvm<5>(); LDNT(b[3], qo + 6144); if (has_w1) PERMPAIR(bt, 2, a[4], a[5]);
    waitvm<5>(); LDNT(b[4], qo + 8192);
    waitvm<5>(); LDNT(b[5], qo + 10240);
    if (has_w1) {
      PERMPAIR(bt, 3, a[6], a[7]);
      lds_wait(C + C_LY, 1);
      const f2 t = gemv_mfma(bt, *(const LAS v4u*)(L + L_YS + 128 * w + fragoff), lane);
      if (!set) *(LAS f2*)(L + L_HPART + partoff) = t;
      lds_signal(CA + C_HP);
    }
    waitvm<5>(); LDNT(b[6], qo + 12288);
    waitvm<5>(); LDNT(b[7], qo + 14336); PERMPAIR(bo, 0, b[0], b[1]);
    waitvm<5>(); LDNT(a[0], q2);
    waitvm<5>(); LDNT(a[1], q2 + 2048); PERMPAIR(bo, 1, b[2], b[3]);
    waitvm<5>(); LDNT(a[2], q2 + 4096);
    waitvm<5>(); LDNT(a[3], q2 + 6144); PERMPAIR(bo, 2, b[4], b[5]);
    waitvm<5>(); LDNT(a[4], q2 + 8192);
    waitvm<5>(); LDNT(a[5], q2 + 10240); PERMPAIR(bo, 3, b[6], b[7]);
    waitvm<5>(); LDNT(a[6], q2 + 12288);
    waitvm<5>(); PERMPAIR(bt, 0, a[0], a[1]);
    waitvm<3>(); PERMPAIR(bt, 1, a[2], a[3]);
    waitvm<1>(); PERMPAIR(bt, 2, a[4], a[5]);
    waitvm<0>();
    a[7] = v4u{0u, 0u, 0u, 0u};
    PERMPAIR(bt, 3, a[6], a[7]);
    f4 d[8];
#pragma unroll
    for (int t = 0; t < 8; ++t) d[t] = f4{0.f, 0.f, 0.f, 0.f};
    lds_wait(C + C_H, 1);
    gemv_acc(bt, *(const LAS v4u*)(L + L_HIMG + fragoff), d);
    lds_wait(C + C_O, 4);
    gemv_acc(bo, *(const LAS v4u*)(L + L_OIMG + fragoff), d);
    const f2 t = gemv_fin(d, lane);
    if (!set) st_sc1(rs, WS_FP + (r * 1024 + 256 * j + 64 * w + 8 * ch + 2 * kg) * 8, v4u{pk24(t.x, t8), t32, pk24(t.y, t8), t32});
    lds_signal(CA + C_FA);
  } else if (wave >= 5) {
    const int ag = wave - 5;
    constexpr float LOG2E = 1.4426950408889634f;
    lds_wait(C + C_LN, 1);
    const u32 t32 = __builtin_amdgcn_readfirstlane(C[C_T32]), t8 = __builtin_amdgcn_readfirstlane(C[C_T8]);
    asm volatile("s_waitcnt vmcnt(0)" : "+v"(cpf)::"memory");
    lds_wait(C + C_QP, 4);
    if (ag == 1) {
      const LAS float* pp = (const LAS float*)(L + L_QPART + lane * 4);
      st_at(p.ws + WS_QP + (j * 4096 + 64 * r + lane) * 8, __float_as_uint((pp[0] + pp[64]) + (pp[128] + pp[192])), t32);
    }
    v4u kf[2][4], vf[4][2];
    const int hd = 8 * g + 4 * hh + kg;
    u32 qoff = L_QIMG + (ci >> 2) * 272 + 16 * kg, mloff = L_MLX + (ci >> 2) * 32 + ag * 8;
    u32 opoff = WS_OP + (((2 * hd + (ag >> 1)) * 32 + sp) * 32 + 16 * (ag & 1) + ci) * 8, mlst = WS_ML + (hd * 32 + sp) * 16;
    {
      v4u v[4];
      const u32 o0 = WS_QP + (1024 * g + 512 * hh + 128 * ag + 2 * lane) * 8;
      hold(C + C_TR, t0, TAU1, 30);
#pragma unroll
      for (int k = 0; k < 4; ++k) v[k] = ld_sc1(rs, o0 + k * 32768);
      __builtin_amdgcn_sched_barrier(0);
      lds_wait(C + C_KV, 4);
#pragma unroll
      for (int T = 0; T < 2; ++T)
#pragma unroll
        for (int t = 0; t < 4; ++t) kf[T][t] = *(const LAS v4u*)(L + L_K + (32 * ag + 16 * T + ci) * 256 + (((4 * t + kg) ^ ci) << 4));
#pragma unroll
      for (int u = 0; u < 4; ++u) {
        u32 vv[8];
#pragma unroll
        for (int e = 0; e < 8; ++e) vv[e] = *(const LAS u32*)(L + L_V + (32 * u + 16 * (e >> 2) + 4 * kg + (e & 3)) * 256 + 64 * ag + 4 * ci);
#pragma unroll
        for (int e = 0; e < 4; ++e) { vf[u][0][e] = plo(vv[2 * e], vv[2 * e + 1]); vf[u][1][e] = phi(vv[2 * e], vv[2 * e + 1]); }
        PIN2(vf[u][0], vf[u][1]);
      }
      asm volatile("" : "+v"(qoff), "+v"(mloff), "+v"(opoff), "+v"(mlst));
      PIN4(kf[0][0], kf[0][1], kf[0][2], kf[0][3]);
      PIN4(kf[1][0], kf[1][1], kf[1][2], kf[1][3]);
      __builtin_amdgcn_sched_barrier(0);
      u32 spins = 0, bad = bad32(v[0], t32) | bad32(v[1], t32) | bad32(v[2], t32) | bad32(v[3], t32);
#pragma unroll 1
      while (bad != 0 && ++spins < SPIN_GLB) {
        __builtin_amdgcn_s_sleep(1);
#pragma unroll
        for (int k = 0; k < 4; ++k) v[k] = ld_sc1(rs, o0 + k * 32768);
        bad = bad32(v[0], t32) | bad32(v[1], t32) | bad32(v[2], t32) | bad32(v[3], t32);
        asm volatile("" ::: "memory");
      }
      const float q0 = (__uint_as_float(v[0].x) + __uint_as_float(v[1].x)) + (__uint_as_float(v[2].x) + __uint_as_float(v[3].x));
      const float q1 = (__uint_as_float(v[0].z) + __uint_as_float(v[1].z)) + (__uint_as_float(v[2].z) + __uint_as_float(v[3].z));
      *(LAS u32*)(L + L_QIMG + ag * 272 + lane * 4) = pkbf(q0, q1);
      lds_signal(CA + C_Q);
    }
    lds_wait(C + C_Q, 4);
    {
      v4u qf[4];
#pragma unroll
      for (int t = 0; t < 4; ++t) qf[t] = *(const LAS v4u*)(L + qoff + 64 * t);
      PIN4(qf[0], qf[1], qf[2], qf[3]);
      f4 s0 = {0.f, 0.f, 0.f, 0.f}, s1 = {0.f, 0.f, 0.f, 0.f};
#pragma unroll
      for (int t = 0; t < 4; ++t) { s0 = mfma(kf[0][t], qf[t], s0); s1 = mfma(kf[1][t], qf[t], s1); }
      constexpr float SC = 0.08838834764831845f;
      s0 = rbf4(s0) * SC; s1 = rbf4(s1) * SC;
      const float m = rbf(max32(max16(fmaxf(fmaxf(fmaxf(s0.x, s0.y), fmaxf(s0.z, s0.w)), fmaxf(fmaxf(s1.x, s1.y), fmaxf(s1.z, s1.w))))));
      s0 = (rbf4(s0) - m) * LOG2E; s1 = (rbf4(s1) - m) * LOG2E;
#pragma unroll
      for (int e = 0; e < 4; ++e) { s0[e] = __builtin_amdgcn_exp2f(s0[e]); s1[e] = __builtin_amdgcn_exp2f(s1[e]); }
      *(LAS v4u*)(L + L_PX + ag * 1024 + lane * 16) = v4u{cvtpk(s0.x, s0.y), cvtpk(s0.z, s0.w), cvtpk(s1.x, s1.y), cvtpk(s1.z, s1.w)};
      const f4 ss = s0 + s1;
      const float l = xrow_sum((ss.x + ss.y) + (ss.z + ss.w));
      if ((lane & 0x33) == 0) *(LAS f2*)(L + mloff) = f2{m, l};
      lds_signal(CA + C_S);
    }
    lds_wait(C + C_S, 4);
    {
      v4u px[4];
      const f4 ml01 = *(const LAS f4*)(L + L_MLX + kg * 32), ml23 = *(const LAS f4*)(L + L_MLX + kg * 32 + 16);
#pragma unroll
      for (int u = 0; u < 4; ++u) px[u] = *(const LAS v4u*)(L + L_PX + u * 1024 + lane * 16);
      PIN4(px[0], px[1], px[2], px[3]);
      const f4 z = {0.f, 0.f, 0.f, 0.f};
      float d0[4], d1[4];
#pragma unroll
      for (int u = 0; u < 4; ++u) { d0[u] = mfma(px[u], vf[u][0], z).x; d1[u] = mfma(px[u], vf[u][1], z).x; }
      const float m = fmaxf(fmaxf(ml01.x, ml01.z), fmaxf(ml23.x, ml23.z));
      const float f0 = __builtin_amdgcn_exp2f((ml01.x - m) * LOG2E), f1 = __builtin_amdgcn_exp2f((ml01.z - m) * LOG2E);
      const float f2_ = __builtin_amdgcn_exp2f((ml23.x - m) * LOG2E), f3 = __builtin_amdgcn_exp2f((ml23.z - m) * LOG2E);
      const float o0 = (f0 * d0[0] + f1 * d0[1]) + (f2_ * d0[2] + f3 * d0[3]);
      const float o1 = (f0 * d1[0] + f1 * d1[1]) + (f2_ * d1[2] + f3 * d1[3]);
      const u32 po = cvtpk(o0, o1), pn = __builtin_amdgcn_mov_dpp(po, XOR1, 0xf, 0xf, true);
      if (!(ci & 1)) st_sc1(rs, opoff, v4u{po, t32, pn, t32});
      if (ag == 0) {
        const float lsum = (f0 * ml01.y + f1 * ml01.w) + (f2_ * ml23.y + f3 * ml23.w);
        if (ci == 0) st_sc1(rs, mlst, v4u{pk24(m, t8), t32, pk24(lsum, t8), t32});
      }
    }
    {
      v4u v0, v1, ml;
      const int sg = lane >> 2;
      const u32 o0 = WS_OP + ((r * 32 + sg) * 32 + 8 * ag + 2 * (lane & 3)) * 8;
      const u32 om = WS_ML + ((r >> 1) * 32 + (lane & 31)) * 16;
      u32 spins = 0, bad;
      hold(C + C_TR, t0, TAU2, 30);
#pragma unroll 1
      do {
        if (spins) __builtin_amdgcn_s_sleep(1);
        ml = ld_sc1(rs, om);
        v0 = ld_sc1(rs, o0); v1 = ld_sc1(rs, o0 + 4096);
        bad = bad40(ml, t32, t8) | bad32(v0, t32) | bad32(v1, t32);
        asm volatile("" ::: "memory");
      } while (bad != 0 && ++spins < SPIN_GLB);
      const float ms = un24(ml.x), ls = un24(ml.z);
      const float M = max16(row_max(ms));
      const float ws_ = __builtin_amdgcn_exp2f((ms - M) * LOG2E);
      const float f0 = __shfl(ws_, sg), f1 = __shfl(ws_, 16 + sg);
      float lt = row_sum(ws_ * ls);
      lt = tr16(lt, lt);
      const float inv = __builtin_amdgcn_rcpf(lt);
      const f4 e4 = unpk4(v2u{v0.x, v0.z}) * f0 + unpk4(v2u{v1.x, v1.z}) * f1;
      float e = tr16(tr32(e4.x, e4.z), tr32(e4.y, e4.w));
      e += dpp<ROR8>(e);
      e += dpp<ROR4>(e);
      e *= inv;
      if ((lane & 12) == 0) *(LAS unsigned short*)(L + L_OIMG + (16 * ag + 4 * (lane & 3) + 2 * (lane >> 5) + (kg & 1)) * 2) = (unsigned short)pkbf(e, e);
      lds_signal(CA + C_O);
    }
  } else {
    asm volatile("s_waitcnt vmcnt(0)" : "+v"(xr[0]), "+v"(xr[1]), "+v"(xr[2]), "+v"(xr[3]), "+v"(eo)::"memory");
    const u64 e = (((u64)eo.y << 32) | eo.x) + 1;
    const u32 t32 = __builtin_amdgcn_readfirstlane((u32)e), t8 = __builtin_amdgcn_readfirstlane((u32)(e >> 32)) & 0xffu;
    {
      f4 v[4] = {unpk4(xr[0]), unpk4(xr[1]), unpk4(xr[2]), unpk4(xr[3])};
      float mb, den, rd;
      if (lane == 0) {
        C[C_T32] = t32; C[C_T8] = t8;
        if (loc == 0) { const u32 tx = now(); st_sc1(rs, WS_ST + xcd * 16, v4u{tx, t32, tx, t32}); }
      }
      ln_stats(v, mb, den, rd);
      const v2u x0 = ln_apply(v[0], mb, den, rd);
      *(LAS v2u*)(L + L_XS + lane * 8) = x0;
      lds_signal(CA + C_LN);
      __builtin_amdgcn_sched_barrier(0);
      v[0] = unpk4(x0);
#pragma unroll
      for (int q = 1; q < 4; ++q) v[q] = unpk4(ln_apply(v[q], mb, den, rd));
      ln_stats(v, mb, den, rd);
      *(LAS v2u*)(L + L_YS + lane * 8) = ln_apply(v[0], mb, den, rd);
      lds_signal(CA + C_LY);
    }
    {
      gate(t0, TRD);
      v4u z0;
      u32 spins = 0, bad;
#pragma unroll 1
      do {
        if (spins) __builtin_amdgcn_s_sleep(10);
        z0 = ld_sc1(rs, WS_ST + (lane & 7) * 16);
        bad = bad32(z0, t32);
        asm volatile("" ::: "memory");
      } while (bad != 0 && ++spins < SPIN_GLB);
      const u32 tb = t0 + (u32)(int)fminf(row_max((float)(int)(z0.x - t0)), 1000.f);
      if (lane == 0) C[C_TRV] = tb;
      lds_signal(CA + C_TR);
    }
    const int li = (lane & 31) < 28 ? (lane & 31) : 27;
    const int cg = 32 * xcd + loc;
    const u32 b1w = *(const GAS u32*)(p.b1 + (56 * r + 2 * li) * 2);
    const u32 xfin = *(const GAS unsigned short*)(p.x + (4 * cg + kg) * 2), bfin = *(const GAS unsigned short*)(p.b2 + (4 * cg + kg) * 2);
    if (has_w1) {
      lds_wait(C + C_HP, 4);
      if (lane < 32) {
        const LAS f2* pp = (const LAS f2*)(L + L_HPART + lane * 8);
        const f2 u = (pp[0] + pp[32]) + (pp[64] + pp[96]);
        st_sc1(rs, WS_HP + (j * 3584 + 64 * sl + 2 * lane) * 8, v4u{__float_as_uint(u.x), t32, __float_as_uint(u.y), t32});
      }
    }
    {
      const u32 o0 = WS_HP + ((lane >> 5) * 3584 + 56 * r + 2 * li) * 8;
      v4u v0, v1;
      u32 spins = 0, bad;
      hold(C + C_TR, t0, TAUH, 40);
#pragma unroll 1
      do {
        if (spins) __builtin_amdgcn_s_sleep(1);
        v0 = ld_sc1(rs, o0); v1 = ld_sc1(rs, o0 + 2 * 3584 * 8);
        bad = bad32(v0, t32) | bad32(v1, t32);
        asm volatile("" ::: "memory");
      } while (bad != 0 && ++spins < SPIN_GLB);
      float h0 = __uint_as_float(v0.x) + __uint_as_float(v1.x), h1 = __uint_as_float(v0.z) + __uint_as_float(v1.z);
      h0 = tr32(h0, h0); h1 = tr32(h1, h1);
      const float hv[2] = {rbf(rbf(h0) + bf2f(b1w & 0xffffu)), rbf(rbf(h1) + __uint_as_float(b1w & 0xffff0000u))};
#pragma unroll
      for (int z = 0; z < 2; ++z) {
        const float t = hv[z], u = 0.7978845608028654f * (t + 0.044715f * t * t * t);
        const float th = 1.f - 2.f / (__expf(2.f * u) + 1.f);
        const int n = 2 * lane + z, st = n >= 28, nn = n - 28 * st, k7 = nn / 7;
        if (lane < 28) *(LAS unsigned short*)(L + L_HIMG + (32 * st + 8 * k7 + (nn - 7 * k7)) * 2) = (unsigned short)pkbf(0.5f * t * (1.f + th), 0.f);
      }
      lds_signal(CA + C_H);
    }
    {
      v4u v0, v1;
      const u32 o0 = WS_FP + (lane * 1024 + 4 * cg) * 8;
      lds_wait(C + C_FA, 4);
      gate(now(), 20);
      u32 spins = 0, bad;
#pragma unroll 1
      do {
        if (spins) __builtin_amdgcn_s_sleep(1);
        v0 = ld_sc1(rs, o0); v1 = ld_sc1(rs, o0 + 16);
        bad = bad40(v0, t32, t8) | bad40(v1, t32, t8);
        asm volatile("" ::: "memory");
      } while (bad != 0 && ++spins < SPIN_GLB);
      const float pa = row_sum(tr16(tr32(un24(v0.x), un24(v1.x)), tr32(un24(v0.z), un24(v1.z))));
      const float res = (bf2f(xfin) + bf2f(bfin)) + pa;
      if (ci == 0) *(GAS unsigned short*)(p.out + (4 * cg + kg) * 2) = (unsigned short)(pkbf(res, res) & 0xffffu);
    }
    if (bid == 0 && lane == 0)
      __hip_atomic_store((GAS u64*)p.ws, e, __ATOMIC_RELAXED, __HIP_MEMORY_SCOPE_AGENT);
  }
}
}

extern "C" void kernel_launch(void* const* d_in, const int* in_sizes, int n_in, void* d_out, int out_size, void* d_ws, size_t ws_size,
                              hipStream_t stream) {
  Params p;
  p.x = (const u8*)d_in[0]; p.kc = (const u8*)d_in[1]; p.vc = (const u8*)d_in[2]; p.wq = (const u8*)d_in[3]; p.wo = (const u8*)d_in[4];
  p.w1 = (const u8*)d_in[6]; p.b1 = (const u8*)d_in[7]; p.w2 = (const u8*)d_in[8]; p.b2 = (const u8*)d_in[9];
  p.out = (u8*)d_out; p.ws = (u8*)d_ws;
  hipLaunchKernelGGL(fused_decode_kernel, dim3(256), dim3(NTHR), 0, stream, p);
}
```
